```python
import torch

def _softmax(x, axis=-1):
    return torch.softmax(x, dim=axis)
def _gelu(x):
    return torch.nn.functional.gelu(x, approximate="tanh")
def _silu(x):
    return torch.nn.functional.silu(x)
def _sigmoid(x):
    return torch.sigmoid(x)
def _layernorm(x, axis=-1, eps=1e-5):
    m = x.mean(dim=axis, keepdim=True)
    v = x.var(dim=axis, keepdim=True, unbiased=False)
    return (x - m) / torch.sqrt(v + eps)
def _scatter_add(tgt, idx, src, axis=0):
    return tgt.clone().index_add_(axis, idx, src)
def _segment_sum(src, seg, n):
    out = torch.zeros((n,) + src.shape[1:], dtype=src.dtype)
    return out.index_add_(0, seg, src)
def _pad(x, pw):
    flat = tuple(v for pair in reversed(pw) for v in pair)
    return torch.nn.functional.pad(x, flat)

kernel_name = 'gated_mix__ssm_conv_s4d__grouped_kv_mqa__prompt'

def setup_inputs():

    _w = (lambda a: (lambda t: t.to(torch.bfloat16) if t.is_floating_point() else t)(torch.as_tensor(a)))
    def _A_setup():
        Bsz, L, D, N = 1, 2048, 576, 128
        return {
            "u": _w((lambda *s: torch.randn(*s, dtype=torch.bfloat16) * 0.05)(Bsz, L, D)),
            "log_A_real": _w((lambda *s: torch.randn(*s, dtype=torch.bfloat16) * 6.4)(D, N) * 0.5),
            "A_imag": _w((lambda *s: torch.randn(*s, dtype=torch.bfloat16) * 0.05)(D, N) * 3.14),
            "C_real": _w((lambda *s: torch.randn(*s, dtype=torch.bfloat16) * 0.05)(D, N) / N ** 0.5),
            "C_imag": _w((lambda *s: torch.randn(*s, dtype=torch.bfloat16) * 0.05)(D, N) / N ** 0.5),
            "log_dt": _w((lambda *s: torch.randn(*s, dtype=torch.bfloat16) * 0.05)(D) * 0.5 - 4.0),
            "D_skip": _w((lambda *s: torch.randn(*s, dtype=torch.bfloat16) * 0.05)(D)),
        }
    _A_in = _A_setup()

    def _B_setup():
        B, S, D, H = 1, 2048, 576, 32
        Dh = D // H
        return {
            "x": _w((lambda *s: torch.randn(*s, dtype=torch.bfloat16) * 0.05)(B, S, D)),
            "wq": _w((lambda *s: torch.randn(*s, dtype=torch.bfloat16) * 0.8)(D, H * Dh)),
            "wk": _w((lambda *s: torch.randn(*s, dtype=torch.bfloat16) * 0.8)(D, Dh)),
            "wv": _w((lambda *s: torch.randn(*s, dtype=torch.bfloat16) * 0.05)(D, Dh)),
            "wo": _w((lambda *s: torch.randn(*s, dtype=torch.bfloat16) * 0.05)(H * Dh, D)),
        }
    _B_in = _B_setup()

    return {
        "A_u": _A_in["u"],
        "A_log_A_real": _A_in["log_A_real"],
        "A_A_imag": _A_in["A_imag"],
        "A_C_real": _A_in["C_real"],
        "A_C_imag": _A_in["C_imag"],
        "A_log_dt": _A_in["log_dt"],
        "A_D_skip": _A_in["D_skip"],
        "B_x": _B_in["x"],
        "B_wq": _B_in["wq"],
        "B_wk": _B_in["wk"],
        "B_wv": _B_in["wv"],
        "B_wo": _B_in["wo"],
        "gate_w": _w((lambda *s: torch.randn(*s, dtype=torch.bfloat16) * 0.05)(576) * 16.0),
    }

def reference(A_u, A_log_A_real, A_A_imag, A_C_real, A_C_imag, A_log_dt, A_D_skip, B_x, B_wq, B_wk, B_wv, B_wo, gate_w):
    def _A_ref(u, log_A_real, A_imag, C_real, C_imag, log_dt, D_skip):
        L = u.shape[1]
        dt = torch.exp(log_dt)
        A = -torch.exp(log_A_real) + 1j * A_imag
        C = (C_real + 1j * C_imag) * dt[:, None]
        pos = (lambda x, dt: x.to(dt))(torch.arange(L), log_dt.dtype)
        P = torch.exp((A * dt[:, None])[:, :, None] * pos[None, None, :])
        K = 2.0 * torch.einsum("dn,dnl->dl", C, P).real
        L2 = 2 * L
        u_t = (lambda x, *d: x.permute(*d))(u, 0, 2, 1)
        Kf = (lambda x, *a, **k: torch.fft.rfft(x.float(), *a, **k))(K, L2)
        Uf = (lambda x, *a, **k: torch.fft.rfft(x.float(), *a, **k))(u_t, L2)
        y = (lambda x, *a, **k: torch.fft.irfft(x, *a, **k).to(torch.bfloat16))(Uf * Kf[None, :, :], L2)[:, :, :L]
        y = (lambda x, *d: x.permute(*d))(y, 0, 2, 1)
        return y + u * D_skip[None, None, :]

    def _B_ref(x, wq, wk, wv, wo):
        B, S, D = x.shape
        H = 32
        Dh = D // H
        q = (lambda x, *d: x.permute(*d))((x @ wq).reshape(B, S, H, Dh), 0, 2, 1, 3)
        k = x @ wk
        v = x @ wv
        scores = torch.einsum("bhqd,bkd->bhqk", q, k) * (Dh ** -0.5)
        p = _softmax(scores, axis=-1)
        o = torch.einsum("bhqk,bkd->bhqd", p, v)
        return (lambda x, *d: x.permute(*d))(o, 0, 2, 1, 3).reshape(B, S, H * Dh) @ wo

    x = A_u
    g = _sigmoid((x * gate_w).sum(-1, keepdims=True))
    a = _A_ref(x, A_log_A_real, A_A_imag, A_C_real, A_C_imag, A_log_dt, A_D_skip)
    b = _B_ref(x, B_wq, B_wk, B_wv, B_wo)
    return g * a + (1 - g) * b
```

```cpp
#include <hip/hip_runtime.h>
#include <hip/hip_bf16.h>
#include <stdint.h>

typedef unsigned short u16;
typedef __attribute__((ext_vector_type(8))) short bf16x8;
typedef __attribute__((ext_vector_type(4))) short s16x4;
typedef __attribute__((ext_vector_type(8))) _Float16 f16x8;
typedef __attribute__((ext_vector_type(2))) _Float16 f16x2;
typedef __attribute__((ext_vector_type(2))) __bf16 bf16x2;
typedef __attribute__((ext_vector_type(2))) float f32x2;
typedef __attribute__((ext_vector_type(4))) float f32x4;
typedef __attribute__((ext_vector_type(16))) float f32x16;
typedef __attribute__((ext_vector_type(4))) unsigned u32x4;
typedef __attribute__((ext_vector_type(2))) unsigned u32x2;

#define DI __device__ __forceinline__
#define LDSP(p) ((__attribute__((address_space(3))) void*)(p))
#define GLBP(p) ((const __attribute__((address_space(1))) void*)(p))
#define MFMA32(a, b, c) __builtin_amdgcn_mfma_f32_32x32x16_bf16((a), (b), (c), 0, 0, 0)

constexpr int SEQ = 2048, DM = 576, NMODE = 128, NHEAD = 32, DHEAD = 18;

constexpr size_t WS_Q = 0;
constexpr size_t WS_O = WS_Q + 2359296;
constexpr size_t WS_UT = WS_O + 2359296;
constexpr size_t WS_KR = WS_UT + 2359296;
constexpr size_t WS_YT = WS_KR + 2359296;
constexpr size_t WS_KV = WS_YT + 2359296;
constexpr size_t WS_G = WS_KV + 262144;

DI float bf2f(u16 b) { return __uint_as_float((unsigned)b << 16); }
DI float bflo(unsigned b) { return __uint_as_float(b << 16); }
DI float bfhi(unsigned b) { return __uint_as_float(b & 0xffff0000u); }
DI unsigned pk_bf16(float lo, float hi) {
  f32x2 f = {lo, hi};
  return __builtin_bit_cast(unsigned, __builtin_convertvector(f, bf16x2));
}
DI unsigned pk_f16(float lo, float hi) {
  f32x2 f = {lo, hi};
  return __builtin_bit_cast(unsigned, __builtin_convertvector(f, f16x2));
}
DI float rbf(float x) { return __uint_as_float(pk_bf16(x, 0.f) << 16); }
DI void glds16(const void* g, void* l) { __builtin_amdgcn_global_load_lds(GLBP(g), LDSP(l), 16, 0, 0); }
DI void wait_vm0() { asm volatile("s_waitcnt vmcnt(0)" ::: "memory"); }
DI void wait_lgkm0() { asm volatile("s_waitcnt lgkmcnt(0)" ::: "memory"); }
DI void barrier_raw() { __builtin_amdgcn_s_barrier(); }
DI s16x4 tr_read(const char* p) {
  return __builtin_amdgcn_ds_read_tr16_b64_v4i16((__attribute__((address_space(3))) s16x4*)(p));
}

template <bool W_IS_A>
DI f32x16 gemm576(const u16* __restrict__ rowp, const char* Ws, int lane) {
  f32x16 acc;
#pragma unroll
  for (int i = 0; i < 16; i++) acc[i] = 0.f;
  const int G = lane >> 4, i16 = lane & 15, q = i16 >> 2, p = i16 & 3;
  const char* bp = Ws + (8 * (G >> 1) + q) * 64 + (16 * (G & 1) + 4 * p) * 2;
  bf16x8 a0[18], a1[18];
#pragma unroll
  for (int s = 0; s < 18; s++) a0[s] = *(const bf16x8*)(rowp + s * 16);
  wait_vm0();
  __syncthreads();
#pragma unroll
  for (int s = 0; s < 18; s++) a1[s] = *(const bf16x8*)(rowp + (18 + s) * 16);
#pragma unroll
  for (int s = 0; s < 18; s++) {
    s16x4 lo = tr_read(bp + s * 1024), hi = tr_read(bp + s * 1024 + 256);
    bf16x8 wv = __builtin_shufflevector(lo, hi, 0, 1, 2, 3, 4, 5, 6, 7);
    acc = W_IS_A ? MFMA32(wv, a0[s], acc) : MFMA32(a0[s], wv, acc);
  }
#pragma unroll
  for (int s = 0; s < 18; s++) {
    s16x4 lo = tr_read(bp + (18 + s) * 1024), hi = tr_read(bp + (18 + s) * 1024 + 256);
    bf16x8 wv = __builtin_shufflevector(lo, hi, 0, 1, 2, 3, 4, 5, 6, 7);
    acc = W_IS_A ? MFMA32(wv, a1[s], acc) : MFMA32(a1[s], wv, acc);
  }
  return acc;
}

DI void stage_w(const u16* __restrict__ W, int n0, char* Ws, int w, int lane) {
#pragma unroll
  for (int m = 0; m < 9; m++) {
    const int mm = w * 9 + m;
    glds16(W + (size_t)(16 * mm + (lane >> 2)) * DM + n0 + (lane & 3) * 8, Ws + mm * 1024);
  }
}

constexpr int K1_LDS = 36864 + 32 * 272;
constexpr int KG_ROW = 528;
constexpr int KG_CH = 32 * KG_ROW + 2688;

struct cplx { float re, im; };
DI cplx cmul(cplx a, cplx b) { return {a.re * b.re - a.im * b.im, a.re * b.im + a.im * b.re}; }

__global__ void __launch_bounds__(256, 3)
k1_prep(const u16* __restrict__ x, const u16* __restrict__ lar, const u16* __restrict__ aim,
        const u16* __restrict__ cre, const u16* __restrict__ cim, const u16* __restrict__ logdt,
        const u16* __restrict__ wq, const u16* __restrict__ wk, const u16* __restrict__ wv,
        const u16* __restrict__ gatew, char* __restrict__ ws) {
  __shared__ __attribute__((aligned(16))) char smem[K1_LDS];
  const int tid = threadIdx.x, lane = tid & 63, w = __builtin_amdgcn_readfirstlane(tid >> 6);
  const int r32 = lane & 31, h = lane >> 5;
  const int b = blockIdx.x;
  if (b < 288) {
    const int rb = b / 18, ct = b % 18, n0 = ct * 32;
    stage_w(wq, n0, smem, w, lane);
    u16* T = (u16*)(smem + 36864);
#pragma unroll
    for (int ps = 0; ps < 2; ps++) {
      const int row = ps * 64 + (tid >> 2), ch = tid & 3;
      bf16x8 v = *(const bf16x8*)(x + (size_t)(128 * rb + row) * DM + n0 + ch * 8);
#pragma unroll
      for (int e = 0; e < 8; e++) T[(ch * 8 + e) * 136 + row] = (u16)v[e];
    }
    const int row = 128 * rb + 32 * w + r32;
    f32x16 acc = gemm576<true>(x + (size_t)row * DM + 8 * h, smem, lane);
    const float c = 0.23570226039551584f * 1.4426950408889634f;
    u16* Qs = (u16*)(ws + WS_Q);
#pragma unroll
    for (int g = 0; g < 4; g++) {
      u32x2 o = {pk_bf16(acc[4 * g] * c, acc[4 * g + 1] * c), pk_bf16(acc[4 * g + 2] * c, acc[4 * g + 3] * c)};
      *(u32x2*)(Qs + (size_t)row * DM + n0 + 8 * g + 4 * h) = o;
    }
    u16* uT = (u16*)(ws + WS_UT);
#pragma unroll
    for (int ps = 0; ps < 2; ps++) {
      const int cc = ps * 16 + (tid >> 4), seg = tid & 15;
      *(u32x4*)(uT + (size_t)(n0 + cc) * SEQ + 128 * rb + seg * 8) = *(const u32x4*)(T + cc * 136 + seg * 8);
    }
  } else if (b < 320) {
    const int bb = b - 288, type = bb & 1, rb = bb >> 1;
    const u16* W = type ? wv : wk;
    for (int k = tid; k < DM; k += 256) {
      const unsigned* src = (const unsigned*)(W + k * DHEAD);
      unsigned d[9];
#pragma unroll
      for (int e = 0; e < 9; e++) d[e] = src[e];
      const unsigned gw = type ? 0u : (unsigned)gatew[k];
      u32x4* dst = (u32x4*)(smem + k * 64);
      dst[0] = u32x4{d[0], d[1], d[2], d[3]};
      dst[1] = u32x4{d[4], d[5], d[6], d[7]};
      dst[2] = u32x4{d[8], gw, 0u, 0u};
      dst[3] = u32x4{0u, 0u, 0u, 0u};
    }
    const int row = 128 * rb + 32 * w + r32;
    char* tile = ws + WS_KV + (size_t)(4 * rb + w) * 4096;
    if (type == 0) {
      f32x16 acc = gemm576<true>(x + (size_t)row * DM + 8 * h, smem, lane);
      *(u32x2*)(tile + r32 * 16 + 8 * h) = u32x2{pk_bf16(acc[0], acc[1]), pk_bf16(acc[2], acc[3])};
      *(u32x2*)(tile + (32 + r32) * 16 + 8 * h) = u32x2{pk_bf16(acc[4], acc[5]), pk_bf16(acc[6], acc[7])};
      u32x4 f1 = {pk_bf16(acc[8], acc[9]), 0x3f803f80u, 0x00003f80u, 0u};
      if (h) f1 = u32x4{0u, 0u, 0u, 0u};
      *(u32x4*)(tile + 1024 + lane * 16) = f1;
      if (!h) {
        const float z = rbf(acc[10]);
        ((float*)(ws + WS_G))[row] = rbf(1.0f / (1.0f + expf(-z)));
      }
    } else {
      f32x16 acc = gemm576<false>(x + (size_t)row * DM + 8 * h, smem, lane);
      u32x4 f2 = {pk_bf16(acc[0], acc[1]), pk_bf16(acc[2], acc[3]), pk_bf16(acc[4], acc[5]), pk_bf16(acc[6], acc[7])};
      u32x4 f3 = {pk_bf16(acc[8], acc[9]), pk_bf16(acc[10], acc[11]), pk_bf16(acc[12], acc[13]), pk_bf16(acc[14], acc[15])};
      if (r32 == DHEAD) {
        f2 = u32x4{0x3f803f80u, 0x3f803f80u, 0x3f803f80u, 0x3f803f80u};
        f3 = f2;
      }
      *(u32x4*)(tile + 2048 + lane * 16) = f2;
      *(u32x4*)(tile + 3072 + lane * 16) = f3;
    }
  } else {
    const int ch = tid >> 7, n = tid & 127, d = (b - 320) * 2 + ch;
    char* base = smem + ch * KG_CH;
    char* Vt = base;
    char* Wt = base + 16 * KG_ROW;
    float* Kc = (float*)(base + 32 * KG_ROW);
    const float dt = rbf(expf(bf2f(logdt[d])));
    const float ar = -rbf(expf(bf2f(lar[d * NMODE + n]))) * dt;
    const float ai = bf2f(aim[d * NMODE + n]) * dt;
    float cr = bf2f(cre[d * NMODE + n]) * dt, ci = bf2f(cim[d * NMODE + n]) * dt;
    float cm = fmaxf(fabsf(cr), fabsf(ci));
#pragma unroll
    for (int o = 32; o >= 1; o >>= 1) cm = fmaxf(cm, __shfl_xor(cm, o));
    if (lane == 0) Kc[660 + (w & 1)] = cm;
    __syncthreads();
    cm = fmaxf(Kc[660], Kc[661]);
    int ex = 0;
    if (cm > 0.f && cm < 3.0e38f) ex = (int)((__float_as_uint(cm) >> 23) & 255u) - 127;
    ex = max(-120, min(120, ex));
    const float sc = __uint_as_float((unsigned)(127 - ex) << 23), isc2 = __uint_as_float((unsigned)(127 + ex + 1) << 23);
    cr *= sc;
    ci *= sc;
    auto zpow = [&](float t) -> cplx {
      const float mag = __builtin_amdgcn_exp2f((ar * t) * 1.4426950408889634f);
      const float rev = __builtin_amdgcn_fractf((ai * t) * 0.15915494309189535f);
      return {mag * __builtin_amdgcn_cosf(rev), mag * __builtin_amdgcn_sinf(rev)};
    };
#pragma unroll
    for (int ti = 0; ti < 4; ti++) {
      const int s = 1 << ti, t0 = ti ? (128 << ti) : 0, nrows = ti == 0 ? 16 : (ti == 3 ? 9 : 8);
      const int toff = ti ? 128 + 128 * ti : 0;
      cplx v = {1.f, 0.f};
      const cplx zs = zpow((float)s);
#pragma unroll
      for (int bb = 0; bb < 16; bb++) {
        *(unsigned*)(Vt + bb * KG_ROW + n * 4) = pk_f16(v.re, v.im);
        v = cmul(v, zs);
      }
      cplx wv_ = {cr, ci};
      if (ti) wv_ = cmul(wv_, zpow((float)t0));
      const cplx zb = zpow((float)(16 * s));
#pragma unroll
      for (int a = 0; a < 16; a++) {
        if (a < nrows) {
          *(unsigned*)(Wt + a * KG_ROW + n * 4) = pk_f16(wv_.re, -wv_.im);
          wv_ = cmul(wv_, zb);
        } else if (ti == 1) {
          *(unsigned*)(Wt + a * KG_ROW + n * 4) = 0u;
        }
      }
      __syncthreads();
      if ((w & 1) == 0) {
        f32x4 acc = {0.f, 0.f, 0.f, 0.f};
        const int off = (lane & 15) * KG_ROW + (lane >> 4) * 16;
#pragma unroll
        for (int st = 0; st < 8; st++) {
          f16x8 af = *(const f16x8*)(Wt + off + st * 64);
          f16x8 bf = *(const f16x8*)(Vt + off + st * 64);
          acc = __builtin_amdgcn_mfma_f32_16x16x32_f16(af, bf, acc, 0, 0, 0);
        }
#pragma unroll
        for (int rg = 0; rg < 4; rg++) {
          const int a = 4 * (lane >> 4) + rg, idx = toff + 16 * a + (lane & 15);
          if (a < nrows && idx <= 640) Kc[idx] = acc[rg] * isc2;
        }
      }
      __syncthreads();
    }
    u16* Kr = (u16*)(ws + WS_KR) + (size_t)d * SEQ;
#pragma unroll
    for (int ps = 0; ps < 2; ps++) {
      const int i0 = (ps * 128 + n) * 8;
      float kv[8];
#pragma unroll
      for (int e = 0; e < 8; e++) {
        const int j = 2047 - (i0 + e);
        const unsigned ub = __float_as_uint((float)j);
        const int bits = (int)((ub + 0x7fffu + ((ub >> 16) & 1u)) >> 16);
        const int idx = j <= 256 ? j : 256 + bits - 0x4380;
        kv[e] = Kc[idx];
      }
      *(u32x4*)(Kr + i0) = u32x4{pk_bf16(kv[0], kv[1]), pk_bf16(kv[2], kv[3]), pk_bf16(kv[4], kv[5]), pk_bf16(kv[6], kv[7])};
    }
  }
}

constexpr int CV_COPY = (8 * 260 + 4) * 16;
constexpr int CV_U = 95 * 80;
constexpr int CV_CH = CV_COPY + CV_U;
constexpr int K2_LDS = 2 * CV_CH;

DI u32x4 exp_pack(const f32x16& s, int o) {
  u32x4 p;
#pragma unroll
  for (int i = 0; i < 4; i++)
    p[i] = pk_bf16(__builtin_amdgcn_exp2f(s[o + 2 * i]), __builtin_amdgcn_exp2f(s[o + 2 * i + 1]));
  return p;
}
DI bf16x8 asbf(u32x4 v) { return __builtin_bit_cast(bf16x8, v); }

DI float tile_max(const f32x16& s) {
  float m = s[0];
#pragma unroll
  for (int i = 1; i < 16; i++) m = fmaxf(m, s[i]);
  return m;
}
DI void set_shift(u32x4& q1, float m, int h) {
  const float nm = -m, hi = rbf(nm), r1 = nm - hi, mid = rbf(r1), lo = rbf(r1 - mid);
  if (!h) {
    q1[1] = pk_bf16(hi, mid);
    q1[2] = pk_bf16(lo, 0.f);
  }
}

__global__ void __launch_bounds__(512, 4)
k2_attn_conv(char* __restrict__ ws) {
  __shared__ __attribute__((aligned(16))) char smem[K2_LDS];
  const int tid = threadIdx.x, lane = tid & 63, w = __builtin_amdgcn_readfirstlane(tid >> 6);
  const int r32 = lane & 31, h = lane >> 5;
  f32x16 zero16;
#pragma unroll
  for (int i = 0; i < 16; i++) zero16[i] = 0.f;
  if (blockIdx.x < 256) {
    const int q = blockIdx.x * 8 + w;
    const char* KV = ws + WS_KV;
    auto issue = [&](int c, int slot) {
      const char* src = KV + c * 16384 + w * 2048 + lane * 16;
      char* dst = smem + slot * 16384 + w * 2048;
      glds16(src, dst);
      glds16(src + 1024, dst + 1024);
    };
    issue(0, 0);
    issue(1, 1);
    const unsigned* qp = (const unsigned*)((const u16*)(ws + WS_Q) + (size_t)q * DM + r32 * DHEAD);
    u32x4 q0 = {qp[4 * h], qp[4 * h + 1], qp[4 * h + 2], qp[4 * h + 3]};
    u32x4 q1 = {h ? 0u : qp[8], 0u, 0u, 0u};
    wait_vm0();
    barrier_raw();
    const char* lp = smem + lane * 16;
    {
      f32x16 s = MFMA32(*(const bf16x8*)(lp), asbf(q0), zero16);
      s = MFMA32(*(const bf16x8*)(lp + 1024), asbf(q1), s);
      float m = tile_max(s);
      m = fmaxf(m, __shfl_xor(m, 32));
      set_shift(q1, m, h);
    }
    f32x16 o = zero16;
    f32x16 s = MFMA32(*(const bf16x8*)(lp), asbf(q0), zero16);
    s = MFMA32(*(const bf16x8*)(lp + 1024), asbf(q1), s);
    int slot = 0;
    for (int c = 0; c < 16; c++) {
      const char* cur = lp + slot * 16384;
      const int nslot = slot == 2 ? 0 : slot + 1;
      const char* nxt = lp + nslot * 16384;
#pragma unroll
      for (int tt = 0; tt < 4; tt++) {
        if (tt == 3) {
          wait_vm0();
          barrier_raw();
          if (c < 14) issue(c + 2, nslot == 2 ? 0 : nslot + 1);
        }
        const char* kp = tt < 3 ? cur + (tt + 1) * 4096 : nxt;
        const bf16x8 k0 = *(const bf16x8*)(kp), k1 = *(const bf16x8*)(kp + 1024);
        const bf16x8 v0 = *(const bf16x8*)(cur + tt * 4096 + 2048), v1 = *(const bf16x8*)(cur + tt * 4096 + 3072);
        f32x16 sn = MFMA32(k0, asbf(q0), zero16);
        sn = MFMA32(k1, asbf(q1), sn);
        const u32x4 p0 = exp_pack(s, 0);
        o = MFMA32(v0, asbf(p0), o);
        const u32x4 p1 = exp_pack(s, 8);
        o = MFMA32(v1, asbf(p1), o);
        s = sn;
      }
      slot = nslot;
    }
    bool bad = false;
#pragma unroll
    for (int i = 0; i < 12; i++) bad |= !(fabsf(o[i]) < 1.0e30f);
    if (__any(bad)) {
      const char* gp = KV + lane * 16;
      q1[1] = 0u;
      q1[2] = 0u;
      float m = -__builtin_inff();
      for (int t = 0; t < 64; t++) {
        f32x16 st = MFMA32(*(const bf16x8*)(gp + t * 4096), asbf(q0), zero16);
        st = MFMA32(*(const bf16x8*)(gp + t * 4096 + 1024), asbf(q1), st);
        m = fmaxf(m, tile_max(st));
      }
      m = fmaxf(m, __shfl_xor(m, 32));
      set_shift(q1, m, h);
      o = zero16;
      for (int t = 0; t < 64; t++) {
        f32x16 st = MFMA32(*(const bf16x8*)(gp + t * 4096), asbf(q0), zero16);
        st = MFMA32(*(const bf16x8*)(gp + t * 4096 + 1024), asbf(q1), st);
        o = MFMA32(*(const bf16x8*)(gp + t * 4096 + 2048), asbf(exp_pack(st, 0)), o);
        o = MFMA32(*(const bf16x8*)(gp + t * 4096 + 3072), asbf(exp_pack(st, 8)), o);
      }
    }
    const float l = __shfl(o[10], r32);
    const float il = 1.0f / l;
    unsigned* op = (unsigned*)((u16*)(ws + WS_O) + (size_t)q * DM + r32 * DHEAD);
    op[2 * h] = pk_bf16(o[0] * il, o[1] * il);
    op[2 * h + 1] = pk_bf16(o[2] * il, o[3] * il);
    op[4 + 2 * h] = pk_bf16(o[4] * il, o[5] * il);
    op[4 + 2 * h + 1] = pk_bf16(o[6] * il, o[7] * il);
    if (!h) op[8] = pk_bf16(o[8] * il, o[9] * il);
  } else {
    const int ch = w >> 2, wq = w & 3, tt = tid & 255;
    const int d = (blockIdx.x - 256) * 2 + ch;
    char* cp = smem + ch * CV_CH;
    char* up = cp + CV_COPY;
    const u32x4* kr = (const u32x4*)((const u16*)(ws + WS_KR) + (size_t)d * SEQ);
    const u32x4 ka = kr[tt];
    u32x4 kb = {0u, 0u, 0u, 0u};
    if (tt < 255) kb = kr[tt + 1];
    const u32x4 uv = ((const u32x4*)((const u16*)(ws + WS_UT) + (size_t)d * SEQ))[tt];
    const unsigned dw[8] = {ka[0], ka[1], ka[2], ka[3], kb[0], kb[1], kb[2], kb[3]};
#pragma unroll
    for (int sft = 0; sft < 8; sft++) {
      const int bsel = 7 - sft, m = sft >> 1;
      u32x4 v;
#pragma unroll
      for (int e = 0; e < 4; e++)
        v[e] = (sft & 1) ? __builtin_amdgcn_alignbit(dw[m + e + 1], dw[m + e], 16) : dw[m + e];
      const int cb = 260 * bsel + (bsel >= 4 ? 4 : 0);
      *(u32x4*)(cp + (cb + tt) * 16) = v;
      if (tt < 4) *(u32x4*)(cp + (cb + 256 + tt) * 16) = u32x4{0u, 0u, 0u, 0u};
    }
    *(u32x4*)(up + (31 + (tt >> 2)) * 80 + (tt & 3) * 16) = uv;
    if (tt < 155) *(u32x4*)(up + tt * 16) = u32x4{0u, 0u, 0u, 0u};
    __syncthreads();
    const int k0 = wq == 0 ? 0 : (wq == 1 ? 12 : (wq == 2 ? 24 : 40));
    const int k1 = wq == 0 ? 12 : (wq == 1 ? 24 : (wq == 2 ? 40 : 64));
    const int a8 = r32 >> 3, b8 = r32 & 7;
    const char* bp = cp + (260 * b8 + (b8 >= 4 ? 4 : 0) + 255 + h - a8) * 16;
    const char* ap = up + (r32 + 31) * 80 + h * 16;
    f32x16 acc0 = zero16, acc1 = zero16;
    for (int k = k0; k < k1; k++) {
      const bf16x8 t0 = *(const bf16x8*)(bp - k * 64), t1 = *(const bf16x8*)(bp - k * 64 + 32);
      const char* a1p = ap + (32 - k) * 80;
      acc1 = MFMA32(*(const bf16x8*)(a1p), t0, acc1);
      acc1 = MFMA32(*(const bf16x8*)(a1p + 32), t1, acc1);
      if (k < 32) {
        const char* a0p = ap - k * 80;
        acc0 = MFMA32(*(const bf16x8*)(a0p), t0, acc0);
        acc0 = MFMA32(*(const bf16x8*)(a0p + 32), t1, acc0);
      }
    }
    __syncthreads();
    float* red = (float*)cp;
#pragma unroll
    for (int g = 0; g < 4; g++) {
      *(f32x4*)(red + ((wq * 8 + g) * 64 + lane) * 4) = f32x4{acc0[4 * g], acc0[4 * g + 1], acc0[4 * g + 2], acc0[4 * g + 3]};
      *(f32x4*)(red + ((wq * 8 + 4 + g) * 64 + lane) * 4) = f32x4{acc1[4 * g], acc1[4 * g + 1], acc1[4 * g + 2], acc1[4 * g + 3]};
    }
    __syncthreads();
    u16* yT = (u16*)(ws + WS_YT) + (size_t)d * SEQ;
#pragma unroll
    for (int tl = 0; tl < 2; tl++) {
      f32x4 sum = *(const f32x4*)(red + ((tl * 4 + wq) * 64 + lane) * 4);
#pragma unroll
      for (int ww = 1; ww < 4; ww++) sum += *(const f32x4*)(red + ((ww * 8 + tl * 4 + wq) * 64 + lane) * 4);
#pragma unroll
      for (int e = 0; e < 4; e++) {
        const int I = 32 * tl + e + 8 * wq + 4 * h;
        yT[32 * I + r32] = (u16)pk_bf16(sum[e], 0.f);
      }
    }
  }
}

__global__ void __launch_bounds__(256, 3)
k3_out(const u16* __restrict__ x, const u16* __restrict__ dskip, const u16* __restrict__ wo,
       const char* __restrict__ ws, u16* __restrict__ out) {
  __shared__ __attribute__((aligned(16))) char smem[36864];
  const int tid = threadIdx.x, lane = tid & 63, w = __builtin_amdgcn_readfirstlane(tid >> 6);
  const int r32 = lane & 31, h = lane >> 5;
  const int rb = blockIdx.x / 18, ct = blockIdx.x % 18, n0 = ct * 32;
  stage_w(wo, n0, smem, w, lane);
  const int row = 128 * rb + 32 * w + r32;
  const u16* yT = (const u16*)(ws + WS_YT);
  const float g = ((const float*)(ws + WS_G))[row];
  u16 yv[16];
  u32x2 uv[4], dv[4];
#pragma unroll
  for (int gi = 0; gi < 4; gi++) {
    const int d0 = n0 + 8 * gi + 4 * h;
#pragma unroll
    for (int e = 0; e < 4; e++) yv[4 * gi + e] = yT[(size_t)(d0 + e) * SEQ + row];
    uv[gi] = *(const u32x2*)(x + (size_t)row * DM + d0);
    dv[gi] = *(const u32x2*)(dskip + d0);
  }
  f32x16 acc = gemm576<true>((const u16*)(ws + WS_O) + (size_t)row * DM + 8 * h, smem, lane);
  const float omg = rbf(1.0f - g);
#pragma unroll
  for (int gi = 0; gi < 4; gi++) {
    float r[4];
#pragma unroll
    for (int e = 0; e < 4; e++) {
      const unsigned uw = uv[gi][e >> 1], dw = dv[gi][e >> 1];
      const float u = (e & 1) ? bfhi(uw) : bflo(uw), ds = (e & 1) ? bfhi(dw) : bflo(dw);
      const float a = rbf(bf2f(yv[4 * gi + e]) + rbf(u * ds));
      const float bb = rbf(acc[4 * gi + e]);
      r[e] = rbf(g * a) + rbf(omg * bb);
    }
    *(u32x2*)(out + (size_t)row * DM + n0 + 8 * gi + 4 * h) = u32x2{pk_bf16(r[0], r[1]), pk_bf16(r[2], r[3])};
  }
}

extern "C" void kernel_launch(void* const* d_in, const int* in_sizes, int n_in, void* d_out, int out_size,
                              void* d_ws, size_t ws_size, hipStream_t stream) {
  const u16* x = (const u16*)d_in[0];
  const u16* lar = (const u16*)d_in[1];
  const u16* aim = (const u16*)d_in[2];
  const u16* cre = (const u16*)d_in[3];
  const u16* cim = (const u16*)d_in[4];
  const u16* logdt = (const u16*)d_in[5];
  const u16* dskip = (const u16*)d_in[6];
  const u16* wq = (const u16*)d_in[8];
  const u16* wk = (const u16*)d_in[9];
  const u16* wv = (const u16*)d_in[10];
  const u16* wo = (const u16*)d_in[11];
  const u16* gatew = (const u16*)d_in[12];
  char* ws = (char*)d_ws;
  k1_prep<<<320 + 288, 256, 0, stream>>>(x, lar, aim, cre, cim, logdt, wq, wk, wv, gatew, ws);
  k2_attn_conv<<<256 + 288, 512, 0, stream>>>(ws);
  k3_out<<<288, 256, 0, stream>>>(x, dskip, wo, ws, (u16*)d_out);
}
```

```cpp
#include <hip/hip_runtime.h>
#include <hip/hip_bf16.h>
#include <stdint.h>

typedef unsigned short u16;
typedef __attribute__((ext_vector_type(8))) short bf16x8;
typedef __attribute__((ext_vector_type(4))) short s16x4;
typedef __attribute__((ext_vector_type(8))) _Float16 f16x8;
typedef __attribute__((ext_vector_type(2))) _Float16 f16x2;
typedef __attribute__((ext_vector_type(2))) __bf16 bf16x2;
typedef __attribute__((ext_vector_type(2))) float f32x2;
typedef __attribute__((ext_vector_type(4))) float f32x4;
typedef __attribute__((ext_vector_type(16))) float f32x16;
typedef __attribute__((ext_vector_type(4))) unsigned u32x4;
typedef __attribute__((ext_vector_type(2))) unsigned u32x2;

#define DI __device__ __forceinline__
#define LDSP(p) ((__attribute__((address_space(3))) void*)(p))
#define GLBP(p) ((const __attribute__((address_space(1))) void*)(p))
#define MFMA32(a, b, c) __builtin_amdgcn_mfma_f32_32x32x16_bf16((a), (b), (c), 0, 0, 0)

constexpr int SEQ = 2048, DM = 576, NMODE = 128, NHEAD = 32, DHEAD = 18;
constexpr int YLD = 2112;

constexpr size_t WS_Q = 0;
constexpr size_t WS_O = WS_Q + 2359296;
constexpr size_t WS_YT = WS_O + 2359296;
constexpr size_t WS_KV = WS_YT + 2433024;
constexpr size_t WS_G = WS_KV + 147456;
constexpr size_t WS_UT = WS_G + 8192;
constexpr size_t WS_KR = WS_UT + 2433024;
constexpr size_t WS_WP = WS_KR + 2433024;

DI float bf2f(u16 b) { return __uint_as_float((unsigned)b << 16); }
DI float bflo(unsigned b) { return __uint_as_float(b << 16); }
DI float bfhi(unsigned b) { return __uint_as_float(b & 0xffff0000u); }
DI unsigned pk_bf16(float lo, float hi) {
  f32x2 f = {lo, hi};
  return __builtin_bit_cast(unsigned, __builtin_convertvector(f, bf16x2));
}
DI unsigned pk_f16(float lo, float hi) {
  f32x2 f = {lo, hi};
  return __builtin_bit_cast(unsigned, __builtin_convertvector(f, f16x2));
}
DI float rbf(float x) { return __uint_as_float(pk_bf16(x, 0.f) << 16); }
DI void glds16(const void* g, void* l) { __builtin_amdgcn_global_load_lds(GLBP(g), LDSP(l), 16, 0, 0); }
template <int N>
DI void wait_vm() { asm volatile("s_waitcnt vmcnt(%0)" ::"n"(N) : "memory"); }
DI void wait_lgkm0() { asm volatile("s_waitcnt lgkmcnt(0)" ::: "memory"); }
DI void barrier_raw() { __builtin_amdgcn_s_barrier(); }
DI s16x4 tr_read(const char* p) {
  return __builtin_amdgcn_ds_read_tr16_b64_v4i16((__attribute__((address_space(3))) s16x4*)(p));
}
DI bf16x8 asbf(u32x4 v) { return __builtin_bit_cast(bf16x8, v); }
DI f32x16 zero16() {
  f32x16 z;
#pragma unroll
  for (int i = 0; i < 16; i++) z[i] = 0.f;
  return z;
}

constexpr int GCH = 17408;
template <bool PACKED>
DI const char* gemm_src(const u16* __restrict__ A, const char* __restrict__ W, int p, int lane) {
  if (p < 8) {
    const int row = 8 * p + (lane >> 3);
    return (const char*)(A + (size_t)row * DM + (((lane & 7) ^ ((row >> 1) & 7)) * 8));
  }
  if (PACKED) return W + (p - 8) * 1024 + lane * 16;
  const int P = 64 * (p - 8) + lane, k = P / 9;
  return W + (size_t)k * (DM * 2) + (P - 9 * k) * 16;
}
template <bool PACKED>
DI size_t gemm_step(int p) { return p < 8 ? 128 : (PACKED ? 9216 : 64 * DM * 2); }
template <bool PACKED>
struct GemmCons {
  int aoff, asw, boff, h;
  bf16x8 fa[2][4], fb[2][4];
  DI GemmCons(int w, int lane) {
    const int wr = w / 3, wc = w % 3, G = lane >> 4, i16 = lane & 15;
    h = lane >> 5;
    const int arow = 32 * wr + (lane & 31);
    aoff = arow * 128;
    asw = (arow >> 1) & 7;
    boff = PACKED ? 8192 + wc * 1024 + (wc == 2 ? ((lane & 7) + 8 * h) * 16 : lane * 16)
                  : 8192 + (8 * (G >> 1) + (i16 >> 2)) * 144 + (32 * wc + 16 * (G & 1) + 4 * (i16 & 3)) * 2;
  }
  DI void ld(const char* buf, int par) {
#pragma unroll
    for (int s = 0; s < 4; s++) {
      fa[par][s] = *(const bf16x8*)(buf + aoff + (((2 * s + h) ^ asw) * 16));
      if (PACKED) {
        fb[par][s] = *(const bf16x8*)(buf + boff + 2304 * s);
      } else {
        s16x4 lo = tr_read(buf + boff + 2304 * s), hi = tr_read(buf + boff + 2304 * s + 576);
        fb[par][s] = __builtin_shufflevector(lo, hi, 0, 1, 2, 3, 4, 5, 6, 7);
      }
    }
  }
  DI void mma(int par, f32x16& acc) {
#pragma unroll
    for (int s = 0; s < 4; s++) acc = MFMA32(fa[par][s], fb[par][s], acc);
  }
};

constexpr int K1_LDS = 81920;
constexpr int KG_ROW = 272;
constexpr int KG_W = 32 * KG_ROW;

DI float smul(float a, float b) { float r; asm("v_mul_f32 %0, %1, %2" : "=v"(r) : "v"(a), "v"(b)); return r; }
DI float sadd(float a, float b) { float r; asm("v_add_f32 %0, %1, %2" : "=v"(r) : "v"(a), "v"(b)); return r; }
struct cplx { float re, im; };
DI cplx cmul(cplx a, cplx b) {
  cplx r;
  asm("v_mul_f32 %0, %2, %4\n\tv_mul_f32 %1, %2, %5\n\tv_fma_f32 %0, -%3, %5, %0\n\tv_fma_f32 %1, %3, %4, %1"
      : "=&v"(r.re), "=&v"(r.im) : "v"(a.re), "v"(a.im), "v"(b.re), "v"(b.im));
  return r;
}

__global__ void __launch_bounds__(512, 4)
k1_prep(const u16* __restrict__ x, const u16* __restrict__ lar, const u16* __restrict__ aim,
        const u16* __restrict__ cre, const u16* __restrict__ cim, const u16* __restrict__ logdt,
        const u16* __restrict__ wq, const u16* __restrict__ wk, const u16* __restrict__ wv,
        const u16* __restrict__ gatew, const u16* __restrict__ wo, char* __restrict__ ws) {
  __shared__ __attribute__((aligned(16))) char smem[K1_LDS];
  const int tid = threadIdx.x, lane = tid & 63, w = __builtin_amdgcn_readfirstlane(tid >> 6);
  const int r32 = lane & 31, h = lane >> 5;
  const int b = blockIdx.x;
  if (b < 256) {
    const int rb = b >> 3, n0 = (b & 7) * 72;
    const u16* A = x + (size_t)rb * 64 * DM;
    const char* W = (const char*)(wq + n0);
#pragma unroll
    for (int j = 0; j < 9; j++) {
      const int e = w + 8 * j;
      if (e < 68) {
        const int kc = e / 17, pp = e - 17 * kc;
        glds16(gemm_src<false>(A, W, pp, lane) + kc * gemm_step<false>(pp), smem + e * 1024);
      }
    }
    f32x16 acc = zero16();
    if (w >= 6) {
      const int L = w - 6;
      const char* src[9];
#pragma unroll
      for (int j = 0; j < 9; j++) src[j] = gemm_src<false>(A, W, (L + 2 * j) > 16 ? 16 : L + 2 * j, lane);
      wait_vm<0>();
      barrier_raw();
#pragma unroll
      for (int c = 1; c < 9; c++) {
        if (c >= 4) {
          const int later = (c + 2 > 8 ? 8 : c + 2) - c;
          if (later == 2) { if (L == 0) wait_vm<18>(); else wait_vm<16>(); }
          else if (later == 1) { if (L == 0) wait_vm<9>(); else wait_vm<8>(); }
          else wait_vm<0>();
        }
        barrier_raw();
        if (c + 3 < 9) {
          char* buf = smem + ((c + 3) & 3) * GCH + L * 1024;
#pragma unroll
          for (int j = 0; j < 9; j++)
            if (j < 8 || L == 0) glds16(src[j] + (size_t)(c + 3) * (j < 4 ? 128 : 64 * DM * 2), buf + j * 2048);
        }
      }
    } else {
      GemmCons<false> g(w, lane);
      wait_vm<0>();
      barrier_raw();
      g.ld(smem, 0);
#pragma unroll
      for (int kc = 0; kc < 9; kc++) {
        if (kc < 8) {
          wait_lgkm0();
          barrier_raw();
          g.ld(smem + ((kc + 1) & 3) * GCH, (kc + 1) & 1);
        }
        g.mma(kc & 1, acc);
      }
    }
    const float c = 0.23570226039551584f * 1.4426950408889634f;
    const int col = 32 * (w % 3) + r32;
    if (w < 6 && col < 72) {
      u16* Qs = (u16*)(ws + WS_Q) + (size_t)(64 * rb + 32 * (w / 3) + 4 * h) * DM + n0 + col;
#pragma unroll
      for (int i = 0; i < 16; i++) Qs[((i & 3) + 8 * (i >> 2)) * DM] = (u16)pk_bf16(acc[i] * c, 0.f);
    }
  } else if (b < 320) {
    const int T = b - 256;
    const u16* xa = x + (size_t)T * 32 * DM;
    for (int p = w; p < 32; p += 8) {
      const int row = 8 * (p & 3) + (lane >> 3);
      glds16(xa + (size_t)row * DM + (p >> 2) * 64 + (((lane & 7) ^ ((row >> 1) & 7)) * 8), smem + p * 1024);
    }
    bf16x8 atail[4];
#pragma unroll
    for (int s = 0; s < 4; s++) atail[s] = *(const bf16x8*)(xa + (size_t)r32 * DM + 512 + 16 * s + 8 * h);
    char* bres = smem + 32768;
    for (int k = tid; k < DM; k += 512) {
      const unsigned* sk = (const unsigned*)(wk + k * DHEAD);
      const unsigned* sv = (const unsigned*)(wv + k * DHEAD);
      unsigned dk[9], dv[9];
#pragma unroll
      for (int e = 0; e < 9; e++) { dk[e] = sk[e]; dv[e] = sv[e]; }
      const unsigned gw = gatew[k];
      u32x4* dst = (u32x4*)(bres + k * 80);
      dst[0] = u32x4{dk[0], dk[1], dk[2], dk[3]};
      dst[1] = u32x4{dk[4], dk[5], dk[6], dk[7]};
      dst[2] = u32x4{dk[8], gw, dv[0], dv[1]};
      dst[3] = u32x4{dv[2], dv[3], dv[4], dv[5]};
      dst[4] = u32x4{dv[6], dv[7], dv[8], 0u};
    }
    if (tid < 2) *(u32x4*)(bres + DM * 80 + tid * 16) = u32x4{0u, 0u, 0u, 0u};
    wait_vm<0>();
    __syncthreads();
    if (w < 2) {
      const int G = lane >> 4, i16 = lane & 15;
      const char* bb = bres + (8 * (G >> 1) + (i16 >> 2)) * 80 + (20 * w + 16 * (G & 1) + 4 * (i16 & 3)) * 2;
      const char* ab = smem + r32 * 128;
      const int asw = (r32 >> 1) & 7;
      f32x16 acc = zero16();
#pragma unroll
      for (int st = 0; st < 36; st++) {
        const bf16x8 av = st < 32 ? *(const bf16x8*)(ab + (st >> 2) * 4096 + (((2 * (st & 3) + h) ^ asw) * 16)) : atail[st & 3];
        s16x4 lo = tr_read(bb + 16 * st * 80), hi = tr_read(bb + (16 * st + 4) * 80);
        const bf16x8 wv_ = __builtin_shufflevector(lo, hi, 0, 1, 2, 3, 4, 5, 6, 7);
        acc = w == 0 ? MFMA32(wv_, av, acc) : MFMA32(av, wv_, acc);
      }
      char* blk = ws + WS_KV + (size_t)(T >> 2) * 9216;
      const int wr = T & 3;
      if (w == 0) {
        char* t0 = blk + wr * 1024;
        *(u32x2*)(t0 + r32 * 16 + 8 * h) = u32x2{pk_bf16(acc[0], acc[1]), pk_bf16(acc[2], acc[3])};
        *(u32x2*)(t0 + (32 + r32) * 16 + 8 * h) = u32x2{pk_bf16(acc[4], acc[5]), pk_bf16(acc[6], acc[7])};
        if (!h) {
          *(unsigned*)(blk + 4096 + wr * 128 + r32 * 4) = pk_bf16(acc[8], acc[9]);
          const float z = rbf(acc[10]);
          ((float*)(ws + WS_G))[32 * T + r32] = rbf(1.0f / (1.0f + expf(-z)));
        }
      } else if (r32 < DHEAD) {
        char* t0 = blk + 4608 + wr * 1152 + h * 288 + r32 * 16;
        *(u32x4*)(t0) = u32x4{pk_bf16(acc[0], acc[1]), pk_bf16(acc[2], acc[3]), pk_bf16(acc[4], acc[5]), pk_bf16(acc[6], acc[7])};
        *(u32x4*)(t0 + 576) = u32x4{pk_bf16(acc[8], acc[9]), pk_bf16(acc[10], acc[11]), pk_bf16(acc[12], acc[13]), pk_bf16(acc[14], acc[15])};
      }
    }
  } else if (b < 464) {
    __builtin_amdgcn_s_setprio(2);
    const int ch = w >> 1, n = 64 * (w & 1) + lane;
    const int d = (b - 320) * 4 + ch;
    char* Vt = smem + w * KG_W;
    char* Wt = Vt + 16 * KG_ROW;
    const float ldt = bf2f(logdt[d]), la = bf2f(lar[d * NMODE + n]), aimv = bf2f(aim[d * NMODE + n]);
    const float cr0 = bf2f(cre[d * NMODE + n]), ci0 = bf2f(cim[d * NMODE + n]);
    const float cr1 = bf2f(cre[d * NMODE + (n ^ 64)]), ci1 = bf2f(cim[d * NMODE + (n ^ 64)]);
    const float dt = rbf(expf(ldt));
    const float ar = -rbf(expf(la)) * dt, ai = aimv * dt;
    float cm = fmaxf(fmaxf(fabsf(cr0), fabsf(ci0)), fmaxf(fabsf(cr1), fabsf(ci1))) * dt;
#pragma unroll
    for (int o = 32; o >= 1; o >>= 1) cm = fmaxf(cm, __shfl_xor(cm, o));
    int ex = 0;
    if (cm > 0.f && cm < 3.0e38f) ex = (int)((__float_as_uint(cm) >> 23) & 255u) - 127;
    ex = max(-120, min(120, ex));
    const float sc = __uint_as_float((unsigned)(127 - ex) << 23), isc2 = __uint_as_float((unsigned)(127 + ex + 1) << 23);
    const cplx cc = {cr0 * dt * sc, ci0 * dt * sc};
    auto zpow = [&](float t) -> cplx {
      const float mag = __builtin_amdgcn_exp2f((ar * t) * 1.4426950408889634f);
      const float rev = __builtin_amdgcn_fractf((ai * t) * 0.15915494309189535f);
      return cplx{mag * __builtin_amdgcn_cosf(rev), mag * __builtin_amdgcn_sinf(rev)};
    };
    cplx zp[11];
#pragma unroll
    for (int i = 0; i < 11; i++) zp[i] = zpow((float)(1 << i));
    f32x4 acc[4];
#pragma unroll
    for (int ti = 0; ti < 4; ti++) {
      const int nrows = ti == 0 ? 16 : (ti == 3 ? 9 : 8);
      cplx v[4];
      v[0] = cplx{1.f, 0.f};
      v[1] = zp[ti];
      v[2] = zp[ti + 1];
      v[3] = cmul(v[2], zp[ti]);
#pragma unroll
      for (int g = 0; g < 4; g++) {
#pragma unroll
        for (int i = 0; i < 4; i++) {
          *(unsigned*)(Vt + (4 * g + i) * KG_ROW + lane * 4) = pk_f16(v[i].re, v[i].im);
          if (g < 3) v[i] = (g == 0 && i == 0) ? zp[ti + 2] : cmul(v[i], zp[ti + 2]);
        }
      }
      cplx u[4];
      u[0] = ti ? cmul(cc, zp[ti + 7]) : cc;
      u[1] = cmul(u[0], zp[ti + 4]);
      u[2] = cmul(u[0], zp[ti + 5]);
      u[3] = cmul(u[2], zp[ti + 4]);
#pragma unroll
      for (int g = 0; g < 4; g++) {
#pragma unroll
        for (int i = 0; i < 4; i++) {
          if (4 * g + i < nrows) {
            *(unsigned*)(Wt + (4 * g + i) * KG_ROW + lane * 4) = pk_f16(u[i].re, -u[i].im);
            if (4 * g + i + 4 < nrows) u[i] = cmul(u[i], zp[ti + 6]);
          } else if (ti == 1) {
            *(unsigned*)(Wt + (4 * g + i) * KG_ROW + lane * 4) = 0u;
          }
        }
      }
      wait_lgkm0();
      const int off = (lane & 15) * KG_ROW + (lane >> 4) * 16;
      acc[ti] = f32x4{0.f, 0.f, 0.f, 0.f};
      f16x8 af[4], bf[4];
#pragma unroll
      for (int st = 0; st < 4; st++) {
        af[st] = *(const f16x8*)(Wt + off + st * 64);
        bf[st] = *(const f16x8*)(Vt + off + st * 64);
      }
      wait_lgkm0();
#pragma unroll
      for (int st = 0; st < 4; st++) acc[ti] = __builtin_amdgcn_mfma_f32_16x16x32_f16(af[st], bf[st], acc[ti], 0, 0, 0);
    }
    float* Kc = (float*)Vt;
#pragma unroll
    for (int ti = 0; ti < 4; ti++) {
      const int nrows = ti == 0 ? 16 : (ti == 3 ? 9 : 8), toff = ti ? 128 + 128 * ti : 0;
#pragma unroll
      for (int rg = 0; rg < 4; rg++) {
        const int a = 4 * (lane >> 4) + rg, idx = toff + 16 * a + (lane & 15);
        if (a < nrows && idx <= 640) Kc[idx] = acc[ti][rg] * isc2;
      }
    }
    wait_lgkm0();
    barrier_raw();
    const float* K0 = (const float*)(smem + (2 * ch) * KG_W);
    const float* K1 = (const float*)(smem + (2 * ch + 1) * KG_W);
#pragma unroll
    for (int ps = 0; ps < 2; ps++) {
      const int i0 = 8 * (n + 128 * ps);
      float kv[8];
#pragma unroll
      for (int e = 0; e < 8; e++) {
        const int j = 2047 - (i0 + e);
        const unsigned ub = __float_as_uint((float)j);
        const int bits = (int)((ub + 0x7fffu + ((ub >> 16) & 1u)) >> 16);
        const int idx = j <= 256 ? j : 256 + bits - 0x4380;
        kv[e] = sadd(K0[idx], K1[idx]);
      }
      *(u32x4*)((u16*)(ws + WS_KR) + (size_t)d * YLD + i0) =
          u32x4{pk_bf16(kv[0], kv[1]), pk_bf16(kv[2], kv[3]), pk_bf16(kv[4], kv[5]), pk_bf16(kv[6], kv[7])};
    }
  } else {
    const int bb_ = b - 464, pr = w >> 1, t2 = tid & 127;
    const int tile = 4 * bb_ + pr, trb = tile / 6, tcb = tile % 6;
    const int unit = bb_ + 48 * pr, ks = unit >> 1, c0 = (unit & 1) * 288;
    const bool has_unit = pr < 2 && unit < 72;
    u16* T = (u16*)(smem + pr * 13824);
    bf16x8 xv[6], wv8[5];
#pragma unroll
    for (int j = 0; j < 6; j++) {
      const int e = t2 + 128 * j;
      xv[j] = *(const bf16x8*)(x + (size_t)(64 * trb + e / 12) * DM + 96 * tcb + (e % 12) * 8);
    }
    if (has_unit) {
#pragma unroll
      for (int j = 0; j < 5; j++) {
        const int e = t2 + 128 * j;
        if (e < 576) wv8[j] = *(const bf16x8*)(wo + (size_t)(16 * ks + e / 36) * DM + c0 + (e % 36) * 8);
      }
    }
#pragma unroll
    for (int j = 0; j < 6; j++) {
      const int e = t2 + 128 * j;
      *(bf16x8*)((char*)T + (e / 12) * 208 + (e % 12) * 16) = xv[j];
    }
    u16* TW = (u16*)(smem + 4 * 13824 + pr * 9472);
    if (has_unit) {
#pragma unroll
      for (int j = 0; j < 5; j++) {
        const int e = t2 + 128 * j;
        if (e < 576) *(bf16x8*)(TW + (e / 36) * 296 + (e % 36) * 8) = wv8[j];
      }
    }
    wait_lgkm0();
    barrier_raw();
    {
      const int G = lane >> 4, i16 = lane & 15, l0 = 32 * (w & 1) + 8 * G;
      const char* rp = (const char*)T + (l0 + (i16 >> 2)) * 208 + (i16 & 3) * 8;
#pragma unroll
      for (int j = 0; j < 6; j++) {
        const s16x4 lo = tr_read(rp + 32 * j), hi = tr_read(rp + 32 * j + 4 * 208);
        *(bf16x8*)((u16*)(ws + WS_UT) + (size_t)(96 * tcb + 16 * j + i16) * YLD + 64 * trb + l0) =
            __builtin_shufflevector(lo, hi, 0, 1, 2, 3, 4, 5, 6, 7);
      }
    }
    if (has_unit) {
#pragma unroll
      for (int j = 0; j < 5; j++) {
        const int e = t2 + 128 * j;
        if (e < 576) {
          const int ctl = e / 144, q = e % 144;
          const int hh = q < 128 ? (q >> 5) & 1 : (q >> 3) & 1;
          const int col = 72 * ctl + (q < 128 ? 32 * (q >> 6) + (q & 31) : 64 + (q & 7));
          unsigned o4[4];
#pragma unroll
          for (int i = 0; i < 4; i++)
            o4[i] = (unsigned)TW[(8 * hh + 2 * i) * 296 + col] | ((unsigned)TW[(8 * hh + 2 * i + 1) * 296 + col] << 16);
          *(u32x4*)(ws + WS_WP + ((size_t)((unit & 1) * 4 + ctl) * 36 + ks) * 2304 + q * 16) = u32x4{o4[0], o4[1], o4[2], o4[3]};
        }
      }
    }
  }
}

constexpr int AT_C = 73728, K2_LDS = 81920;
constexpr int CV_COPY = 4 * 4160;
constexpr int CV2_CH = 40960;

DI u32x4 exp_pack(const f32x16& s, int o) {
  u32x4 p;
#pragma unroll
  for (int i = 0; i < 4; i++)
    p[i] = pk_bf16(__builtin_amdgcn_exp2f(s[o + 2 * i]), __builtin_amdgcn_exp2f(s[o + 2 * i + 1]));
  return p;
}
DI float tile_max(const f32x16& s) {
  float m = s[0];
#pragma unroll
  for (int i = 1; i < 16; i++) m = fmaxf(m, s[i]);
  return m;
}
DI void set_shift(u32x4& q1, float m, int h) {
  const float nm = -m, hi = rbf(nm), r1 = nm - hi, mid = rbf(r1), lo = rbf(r1 - mid);
  if (!h) {
    q1[1] = pk_bf16(hi, mid);
    q1[2] = pk_bf16(lo, 0.f);
  }
}
#define SCHED_FENCE() __builtin_amdgcn_sched_barrier(0)

template <int NPER>
DI void conv_pass(char* __restrict__ ws, char* smem, int dbase, int w, int tid, int lane) {
  constexpr int TPC = NPER * 64;
  const int r32 = lane & 31, h = lane >> 5;
  const int ch = w / NPER, wq = w % NPER, tt = tid % TPC;
  const int d = dbase + ch;
  char* base = smem + ch * CV2_CH;
  __builtin_amdgcn_s_setprio(3);
  if (tt < 256) {
    const u32x4* kr = (const u32x4*)((const u16*)(ws + WS_KR) + (size_t)d * YLD);
    const u32x4 ka = kr[tt];
    u32x2 kb = *(const u32x2*)(kr + tt + 1);
    if (tt == 255) kb = u32x2{0u, 0u};
    const u32x4 uv = ((const u32x4*)((const u16*)(ws + WS_UT) + (size_t)d * YLD))[tt];
    const unsigned dw[6] = {ka[0], ka[1], ka[2], ka[3], kb[0], kb[1]};
#pragma unroll
    for (int sft = 0; sft < 4; sft++) {
      u32x4 v;
#pragma unroll
      for (int e = 0; e < 4; e++)
        v[e] = (sft & 1) ? __builtin_amdgcn_alignbit(dw[(sft >> 1) + e + 1], dw[(sft >> 1) + e], 16) : dw[(sft >> 1) + e];
      char* cpy = base + (3 - sft) * 4160;
      *(u32x4*)(cpy + tt * 16) = v;
      if (tt < 4) *(u32x4*)(cpy + 4096 + tt * 16) = u32x4{0u, 0u, 0u, 0u};
    }
    if (tt < 155) *(u32x4*)(base + CV_COPY + tt * 16) = u32x4{0u, 0u, 0u, 0u};
    *(u32x4*)(base + CV_COPY + (31 + (tt >> 2)) * 80 + (tt & 3) * 16) = uv;
  }
  __syncthreads();
  if (NPER == 4) __builtin_amdgcn_s_setprio(0);
  int k0, k1;
  if (NPER == 4) {
    k0 = wq == 0 ? 0 : (wq == 1 ? 12 : (wq == 2 ? 24 : 40));
    k1 = wq == 0 ? 12 : (wq == 1 ? 24 : (wq == 2 ? 40 : 64));
  } else {
    k0 = wq < 6 ? 6 * wq : (wq == 6 ? 40 : 52);
    k1 = wq < 5 ? 6 * wq + 6 : (wq == 5 ? 40 : (wq == 6 ? 52 : 64));
  }
  const char* bp = base + (r32 & 3) * 4160 + (511 + 2 * h - (r32 >> 2)) * 8;
  const char* ap = base + CV_COPY + (r32 + 31) * 80 + h * 16;
  f32x16 acc0 = zero16(), acc1 = zero16();
  for (int kb = k0; kb < k1; kb += 2) {
    bf16x8 t0[2], t1[2], a1a[2], a1b[2];
#pragma unroll
    for (int i = 0; i < 2; i++) {
      const char* tp = bp - (kb + i) * 64;
      const u32x2 ta = *(const u32x2*)(tp), tb = *(const u32x2*)(tp + 8);
      const u32x2 tc = *(const u32x2*)(tp + 32), td = *(const u32x2*)(tp + 40);
      t0[i] = asbf(u32x4{ta[0], ta[1], tb[0], tb[1]});
      t1[i] = asbf(u32x4{tc[0], tc[1], td[0], td[1]});
      const char* a1p = ap + (32 - kb - i) * 80;
      a1a[i] = *(const bf16x8*)(a1p);
      a1b[i] = *(const bf16x8*)(a1p + 32);
    }
    if (kb < 32) {
      bf16x8 a0a[2], a0b[2];
#pragma unroll
      for (int i = 0; i < 2; i++) {
        const char* a0p = ap - (kb + i) * 80;
        a0a[i] = *(const bf16x8*)(a0p);
        a0b[i] = *(const bf16x8*)(a0p + 32);
      }
#pragma unroll
      for (int i = 0; i < 2; i++) {
        acc1 = MFMA32(a1a[i], t0[i], acc1);
        acc0 = MFMA32(a0a[i], t0[i], acc0);
        acc1 = MFMA32(a1b[i], t1[i], acc1);
        acc0 = MFMA32(a0b[i], t1[i], acc0);
      }
    } else {
#pragma unroll
      for (int i = 0; i < 2; i++) {
        acc1 = MFMA32(a1a[i], t0[i], acc1);
        acc1 = MFMA32(a1b[i], t1[i], acc1);
      }
    }
  }
  __builtin_amdgcn_s_setprio(3);
  __syncthreads();
  float* red = (float*)base;
#pragma unroll
  for (int g = 0; g < 4; g++) {
    *(f32x4*)(red + ((wq * 8 + g) * 64 + lane) * 4) = f32x4{acc0[4 * g], acc0[4 * g + 1], acc0[4 * g + 2], acc0[4 * g + 3]};
    *(f32x4*)(red + ((wq * 8 + 4 + g) * 64 + lane) * 4) = f32x4{acc1[4 * g], acc1[4 * g + 1], acc1[4 * g + 2], acc1[4 * g + 3]};
  }
  __syncthreads();
  u16* yT = (u16*)(ws + WS_YT) + (size_t)d * 64;
#pragma unroll
  for (int it = 0; it < 8 / NPER; it++) {
    const int tl = NPER == 4 ? it : wq >> 2, g = wq & 3;
    f32x4 sum = *(const f32x4*)(red + ((tl * 4 + g) * 64 + lane) * 4);
#pragma unroll
    for (int ww = 1; ww < NPER; ww++) sum += *(const f32x4*)(red + ((ww * 8 + tl * 4 + g) * 64 + lane) * 4);
#pragma unroll
    for (int e = 0; e < 4; e++) {
      const int I = 32 * tl + e + 8 * g + 4 * h;
      yT[(size_t)(I >> 1) * (DM * 64) + (I & 1) * 32 + r32] = (u16)pk_bf16(sum[e], 0.f);
    }
  }
}

__global__ void __launch_bounds__(512, 4)
k2_attn_conv(char* __restrict__ ws) {
  __shared__ __attribute__((aligned(16))) char smem[K2_LDS];
  const int tid = threadIdx.x, lane = tid & 63, w = __builtin_amdgcn_readfirstlane(tid >> 6);
  const int r32 = lane & 31, h = lane >> 5;
  if (blockIdx.x < 256) {
    const int q = blockIdx.x * 8 + w;
    const u16* qp = (const u16*)(ws + WS_Q) + (size_t)q * DM + r32 * DHEAD;
    u32x4 q0;
    unsigned q16;
    asm volatile("global_load_dwordx4 %0, %2, off\n\tglobal_load_dword %1, %3, off"
                 : "=&v"(q0), "=&v"(q16) : "v"(qp + 8 * h), "v"(qp + 16) : "memory");
    const char* src = ws + WS_KV + w * 1024 + lane * 16;
    auto issue = [&](int j) { glds16(src + j * 8192, smem + w * 1024 + (j >= 9 ? j - 9 : j) * 8192); };
    issue(0);
    issue(1);
    issue(2);
    if (tid < 8) {
      *(u32x4*)(smem + AT_C + tid * 576) = u32x4{0x3f803f80u, 0x3f803f80u, 0x3f803f80u, 0x3f803f80u};
      *(u32x4*)(smem + AT_C + tid * 576 + 16) = u32x4{0u, 0u, 0u, 0u};
      if (tid < 4) *(unsigned*)(smem + AT_C + 32 + tid * 128) = 0u;
    }
    asm volatile("s_waitcnt vmcnt(0) lgkmcnt(0)" : "+v"(q0), "+v"(q16)::"memory");
    barrier_raw();
    u32x4 q1 = {h ? 0u : q16, 0u, 0u, 0u};
    const unsigned kc1 = h ? 0u : 0x3f803f80u, kc2 = h ? 0u : 0x00003f80u;
    const int k1step = h ? 0 : 9216, vstep = r32 < DHEAD ? 9216 : 0;
    const int k0base = lane * 16;
    const int k1base = h ? AT_C + 32 : 4096 + r32 * 4;
    const int vbase = r32 < DHEAD ? 4608 + h * 288 + r32 * 16 : (r32 == DHEAD ? AT_C : AT_C + 16);
    auto ldk0 = [&](int a, int t) { return *(const bf16x8*)(smem + a + t * 1024); };
    auto ldk1 = [&](int a, int t) { return asbf(u32x4{*(const unsigned*)(smem + a + t * 128), kc1, kc2, 0u}); };
    auto ldv = [&](int a, int t, int s) { return *(const bf16x8*)(smem + a + t * 1152 + s * 576); };

    bf16x8 k0 = ldk0(k0base, 0), k1 = ldk1(k1base, 0);
    {
      f32x16 s = MFMA32(k0, asbf(q0), zero16());
      s = MFMA32(k1, asbf(q1), s);
      float m = tile_max(s);
      m = fmaxf(m, __shfl_xor(m, 32));
      set_shift(q1, m, h);
    }
    f32x16 o = zero16();
    f32x16 s = MFMA32(k0, asbf(q0), zero16());
    s = MFMA32(k1, asbf(q1), s);
    k0 = ldk0(k0base, 1);
    k1 = ldk1(k1base, 1);
    bf16x8 v0 = ldv(vbase, 0, 0), v1 = ldv(vbase, 0, 1);
    int ak0 = k0base, ak1 = k1base, av = vbase;
#pragma unroll
    for (int c = 0; c < 16; c++) {
      const int ns = c == 15 ? 7 : ((c + 1) & 7);
      const int nk0 = k0base + ns * 9216, nk1 = k1base + ns * k1step, nv = vbase + ns * vstep;
#pragma unroll
      for (int tt = 0; tt < 4; tt++) {
        const int T = 4 * c + tt;
        u32x4 p0, p1;
        p0[0] = pk_bf16(__builtin_amdgcn_exp2f(s[0]), __builtin_amdgcn_exp2f(s[1]));
        p0[1] = pk_bf16(__builtin_amdgcn_exp2f(s[2]), __builtin_amdgcn_exp2f(s[3]));
        SCHED_FENCE();
        f32x16 sn = MFMA32(k0, asbf(q0), zero16());
        SCHED_FENCE();
        p0[2] = pk_bf16(__builtin_amdgcn_exp2f(s[4]), __builtin_amdgcn_exp2f(s[5]));
        p0[3] = pk_bf16(__builtin_amdgcn_exp2f(s[6]), __builtin_amdgcn_exp2f(s[7]));
        SCHED_FENCE();
        o = MFMA32(v0, asbf(p0), o);
        SCHED_FENCE();
        p1[0] = pk_bf16(__builtin_amdgcn_exp2f(s[8]), __builtin_amdgcn_exp2f(s[9]));
        p1[1] = pk_bf16(__builtin_amdgcn_exp2f(s[10]), __builtin_amdgcn_exp2f(s[11]));
        SCHED_FENCE();
        sn = MFMA32(k1, asbf(q1), sn);
        if (T == 6) {
          wait_vm<2>();
          barrier_raw();
        } else if (T == 22 || T == 30 || T == 38 || T == 54) {
          wait_vm<0>();
          barrier_raw();
        }
        if (T < 6) issue(3 + T);
        if (T >= 22 && T < 27) issue(T - 13);
        if (T == 30 || T == 31) issue(T - 16);
        if (T == 38 || T == 39) issue(T - 22);
        k0 = tt < 2 ? ldk0(ak0, tt + 2) : ldk0(nk0, tt - 2);
        k1 = tt < 2 ? ldk1(ak1, tt + 2) : ldk1(nk1, tt - 2);
        v0 = tt < 3 ? ldv(av, tt + 1, 0) : ldv(nv, 0, 0);
        const bf16x8 vn1 = tt < 3 ? ldv(av, tt + 1, 1) : ldv(nv, 0, 1);
        SCHED_FENCE();
        p1[2] = pk_bf16(__builtin_amdgcn_exp2f(s[12]), __builtin_amdgcn_exp2f(s[13]));
        p1[3] = pk_bf16(__builtin_amdgcn_exp2f(s[14]), __builtin_amdgcn_exp2f(s[15]));
        SCHED_FENCE();
        o = MFMA32(v1, asbf(p1), o);
        SCHED_FENCE();
        v1 = vn1;
        s = sn;
      }
      ak0 = nk0;
      ak1 = nk1;
      av = nv;
    }
    bool bad = false;
#pragma unroll
    for (int i = 0; i < 12; i++) bad |= !(fabsf(o[i]) < 1.0e30f);
    if (__any(bad)) {
      const char* g = ws + WS_KV;
      auto gk0 = [&](int t) { return *(const bf16x8*)(g + (t >> 2) * 9216 + (t & 3) * 1024 + lane * 16); };
      auto gk1 = [&](int t) {
        const unsigned v = *(const unsigned*)(g + (t >> 2) * 9216 + 4096 + (t & 3) * 128 + r32 * 4);
        return asbf(u32x4{h ? 0u : v, kc1, kc2, 0u});
      };
      auto gv = [&](int t, int st) {
        const int dd = r32 < DHEAD ? r32 : 0;
        u32x4 v = *(const u32x4*)(g + (t >> 2) * 9216 + 4608 + (t & 3) * 1152 + st * 576 + h * 288 + dd * 16);
        const unsigned cv = r32 == DHEAD ? 0x3f803f80u : 0u;
        if (r32 >= DHEAD) v = u32x4{cv, cv, cv, cv};
        return asbf(v);
      };
      q1[1] = 0u;
      q1[2] = 0u;
      float m = -__builtin_inff();
      for (int t = 0; t < 64; t++) {
        f32x16 st = MFMA32(gk0(t), asbf(q0), zero16());
        st = MFMA32(gk1(t), asbf(q1), st);
        m = fmaxf(m, tile_max(st));
      }
      m = fmaxf(m, __shfl_xor(m, 32));
      set_shift(q1, m, h);
      o = zero16();
      for (int t = 0; t < 64; t++) {
        f32x16 st = MFMA32(gk0(t), asbf(q0), zero16());
        st = MFMA32(gk1(t), asbf(q1), st);
        o = MFMA32(gv(t, 0), asbf(exp_pack(st, 0)), o);
        o = MFMA32(gv(t, 1), asbf(exp_pack(st, 8)), o);
      }
    }
    const float l = __shfl(o[10], r32);
    const float il = 1.0f / l;
    unsigned* op = (unsigned*)((u16*)(ws + WS_O) + (size_t)q * DM + r32 * DHEAD);
    op[2 * h] = pk_bf16(o[0] * il, o[1] * il);
    op[2 * h + 1] = pk_bf16(o[2] * il, o[3] * il);
    op[4 + 2 * h] = pk_bf16(o[4] * il, o[5] * il);
    op[4 + 2 * h + 1] = pk_bf16(o[6] * il, o[7] * il);
    if (!h) op[8] = pk_bf16(o[8] * il, o[9] * il);
  } else {
    const int cb = blockIdx.x - 256;
    conv_pass<4>(ws, smem, 2 * cb, w, tid, lane);
    if (cb < 64) {
      wait_lgkm0();
      barrier_raw();
      conv_pass<8>(ws, smem, 512 + cb, w, tid, lane);
    }
  }
}

constexpr int K3_G = 9 * GCH, K3_LDS = K3_G + 512;

__global__ void __launch_bounds__(768, 3)
k3_out(const u16* __restrict__ x, const u16* __restrict__ dskip, const char* __restrict__ ws, u16* __restrict__ out) {
  __shared__ __attribute__((aligned(16))) char smem[K3_LDS];
  const int tid = threadIdx.x, lane = tid & 63, w = __builtin_amdgcn_readfirstlane(tid >> 6);
  const int r32 = lane & 31, h = lane >> 5;
  const int rb = blockIdx.x >> 3, n0 = (blockIdx.x & 7) * 72;
  const int wr = (w / 3) & 1, wc = w % 3;
  f32x16 acc = zero16();
  if (w >= 6) {
    const int L = w - 6;
    const u16* A = (const u16*)(ws + WS_O) + (size_t)rb * 64 * DM;
    const char* W = ws + WS_WP + (size_t)(blockIdx.x & 7) * 82944;
    if (L == 5 && lane < 16) glds16(ws + WS_G + (size_t)rb * 256 + lane * 16, smem + K3_G);
    if (L == 5 && lane < 9) glds16(dskip + n0 + lane * 8, smem + K3_G + 256);
    const char* src[3];
    size_t step[3];
#pragma unroll
    for (int j = 0; j < 3; j++) {
      const int pp = L + 6 * j > 16 ? L : L + 6 * j;
      src[j] = gemm_src<true>(A, W, pp, lane);
      step[j] = gemm_step<true>(pp);
    }
    auto issue = [&](int kc) {
#pragma unroll
      for (int j = 0; j < 3; j++)
        glds16(src[j] + kc * step[j], smem + kc * GCH + (L + 6 * j > 16 ? L : L + 6 * j) * 1024);
    };
#pragma unroll
    for (int kc = 0; kc < 4; kc++) issue(kc);
    wait_vm<6>();
    barrier_raw();
    issue(4);
    issue(5);
    wait_vm<6>();
    barrier_raw();
    issue(6);
    issue(7);
    issue(8);
    wait_vm<9>();
    barrier_raw();
#pragma unroll
    for (int j = 0; j < 3; j++) {
      const int p = L + 6 * j;
      if (p < 9) {
        const int dd = 8 * p + (lane >> 3);
        glds16((const u16*)(ws + WS_YT) + ((size_t)rb * DM + n0 + dd) * 64 + (lane & 7) * 8, smem + p * 1024);
      } else {
        const int P = 64 * (p - 9) + lane, l = P / 9;
        glds16(x + (size_t)(64 * rb + l) * DM + n0 + (P - 9 * l) * 8, smem + GCH + (p - 9) * 1024);
      }
    }
    wait_vm<3>();
    barrier_raw();
    wait_vm<0>();
  } else {
    GemmCons<true> g(w, lane);
#pragma unroll
    for (int kc = 0; kc < 9; kc++) {
      if (kc == 0 || kc == 2 || kc == 4 || kc == 6) {
        wait_lgkm0();
        barrier_raw();
        g.ld(smem + kc * GCH, kc & 1);
      }
      if (kc == 0 || kc == 2 || kc == 4 || kc == 6 || kc == 7) g.ld(smem + (kc + 1) * GCH, (kc + 1) & 1);
      g.mma(kc & 1, acc);
    }
    const int dl = 32 * wc + r32;
    if (dl < 72) {
      u16* bt = (u16*)(smem + 2 * GCH) + (32 * wr + 4 * h) * 72 + dl;
#pragma unroll
      for (int i = 0; i < 16; i++) bt[((i & 3) + 8 * (i >> 2)) * 72] = (u16)pk_bf16(acc[i], 0.f);
    }
    wait_lgkm0();
  }
  barrier_raw();
  if (tid < 576) {
    const int l = tid / 9, c8 = (tid - 9 * l) * 8;
    const u32x4 bv = *(const u32x4*)(smem + 2 * GCH + l * 144 + c8 * 2);
    const u32x4 uv = *(const u32x4*)(smem + GCH + l * 144 + c8 * 2);
    const u32x4 dv = *(const u32x4*)(smem + K3_G + 256 + c8 * 2);
    const float gg = *(const float*)(smem + K3_G + l * 4), omg = rbf(1.0f - gg);
    float r[8];
#pragma unroll
    for (int e = 0; e < 8; e++) {
      const float y = bf2f(*(const u16*)(smem + (c8 + e) * 128 + l * 2));
      const float u = (e & 1) ? bfhi(uv[e >> 1]) : bflo(uv[e >> 1]);
      const float ds = (e & 1) ? bfhi(dv[e >> 1]) : bflo(dv[e >> 1]);
      const float bb = (e & 1) ? bfhi(bv[e >> 1]) : bflo(bv[e >> 1]);
      const float a = rbf(sadd(y, rbf(smul(u, ds))));
      r[e] = sadd(rbf(smul(gg, a)), rbf(smul(omg, bb)));
    }
    *(u32x4*)(out + (size_t)(64 * rb + l) * DM + n0 + c8) =
        u32x4{pk_bf16(r[0], r[1]), pk_bf16(r[2], r[3]), pk_bf16(r[4], r[5]), pk_bf16(r[6], r[7])};
  }
}

extern "C" void kernel_launch(void* const* d_in, const int* in_sizes, int n_in, void* d_out, int out_size,
                              void* d_ws, size_t ws_size, hipStream_t stream) {
  const u16* x = (const u16*)d_in[0];
  const u16* lar = (const u16*)d_in[1];
  const u16* aim = (const u16*)d_in[2];
  const u16* cre = (const u16*)d_in[3];
  const u16* cim = (const u16*)d_in[4];
  const u16* logdt = (const u16*)d_in[5];
  const u16* dskip = (const u16*)d_in[6];
  const u16* wq = (const u16*)d_in[8];
  const u16* wk = (const u16*)d_in[9];
  const u16* wv = (const u16*)d_in[10];
  const u16* wo = (const u16*)d_in[11];
  const u16* gatew = (const u16*)d_in[12];
  char* ws = (char*)d_ws;
  k1_prep<<<320 + 192, 512, 0, stream>>>(x, lar, aim, cre, cim, logdt, wq, wk, wv, gatew, wo, ws);
  k2_attn_conv<<<256 + 256, 512, 0, stream>>>(ws);
  k3_out<<<256, 768, 0, stream>>>(x, dskip, ws, (u16*)d_out);
}
```

```cpp
#include <hip/hip_runtime.h>
#include <hip/hip_bf16.h>
#include <stdint.h>

typedef unsigned short u16;
typedef __attribute__((ext_vector_type(8))) short bf16x8;
typedef __attribute__((ext_vector_type(4))) short s16x4;
typedef __attribute__((ext_vector_type(8))) _Float16 f16x8;
typedef __attribute__((ext_vector_type(2))) _Float16 f16x2;
typedef __attribute__((ext_vector_type(2))) __bf16 bf16x2;
typedef __attribute__((ext_vector_type(2))) float f32x2;
typedef __attribute__((ext_vector_type(4))) float f32x4;
typedef __attribute__((ext_vector_type(16))) float f32x16;
typedef __attribute__((ext_vector_type(4))) unsigned u32x4;
typedef __attribute__((ext_vector_type(2))) unsigned u32x2;

#define DI __device__ __forceinline__
#define LDSP(p) ((__attribute__((address_space(3))) void*)(p))
#define GLBP(p) ((const __attribute__((address_space(1))) void*)(p))
#define MFMA32(a, b, c) __builtin_amdgcn_mfma_f32_32x32x16_bf16((a), (b), (c), 0, 0, 0)
#define SCHED_FENCE() __builtin_amdgcn_sched_barrier(0)

constexpr int SEQ = 2048, DM = 576, NMODE = 128, DHEAD = 18;
constexpr int YLD = 2112;

constexpr size_t WS_Q = 0;
constexpr size_t WS_O = WS_Q + 2359296;
constexpr size_t WS_YT = WS_O + 2359296;
constexpr size_t WS_KV = WS_YT + 2359296;
constexpr size_t WS_G = WS_KV + 147456;
constexpr size_t WS_WP = WS_G + 8192;
constexpr size_t WS_UT = WS_WP + 663552;
constexpr size_t WS_KR = WS_UT + 2433024;

DI float bf2f(u16 b) { return __uint_as_float((unsigned)b << 16); }
DI float bflo(unsigned b) { return __uint_as_float(b << 16); }
DI float bfhi(unsigned b) { return __uint_as_float(b & 0xffff0000u); }
DI unsigned pk_bf16(float lo, float hi) {
  f32x2 f = {lo, hi};
  return __builtin_bit_cast(unsigned, __builtin_convertvector(f, bf16x2));
}
DI unsigned pk_f16(float lo, float hi) {
  f32x2 f = {lo, hi};
  return __builtin_bit_cast(unsigned, __builtin_convertvector(f, f16x2));
}
DI float rbf(float x) { return __uint_as_float(pk_bf16(x, 0.f) << 16); }
DI void glds16(const void* g, void* l) { __builtin_amdgcn_global_load_lds(GLBP(g), LDSP(l), 16, 0, 0); }
template <int N>
DI void wait_vm() { asm volatile("s_waitcnt vmcnt(%0)" ::"n"(N) : "memory"); }
DI void wait_lgkm0() { asm volatile("s_waitcnt lgkmcnt(0)" ::: "memory"); }
DI void barrier_raw() { __builtin_amdgcn_s_barrier(); }
DI void lds_barrier() { wait_lgkm0(); barrier_raw(); }
DI s16x4 tr_read(const char* p) {
  return __builtin_amdgcn_ds_read_tr16_b64_v4i16((__attribute__((address_space(3))) s16x4*)(p));
}
DI bf16x8 asbf(u32x4 v) { return __builtin_bit_cast(bf16x8, v); }
DI f32x16 zero16() {
  f32x16 z;
#pragma unroll
  for (int i = 0; i < 16; i++) z[i] = 0.f;
  return z;
}
DI void st16(void* p, u32x4 v) { asm volatile("global_store_dwordx4 %0, %1, off sc1\n\ts_nop 1" ::"v"(p), "v"(v) : "memory"); }
DI void st8(void* p, u32x2 v) { asm volatile("global_store_dwordx2 %0, %1, off sc1" ::"v"(p), "v"(v) : "memory"); }
DI void st4(void* p, unsigned v) { asm volatile("global_store_dword %0, %1, off sc1" ::"v"(p), "v"(v) : "memory"); }
DI void st2(void* p, unsigned v) { asm volatile("global_store_short %0, %1, off sc1" ::"v"(p), "v"(v) : "memory"); }
DI float smul(float a, float b) { float r; asm("v_mul_f32 %0, %1, %2" : "=v"(r) : "v"(a), "v"(b)); return r; }
DI float sadd(float a, float b) { float r; asm("v_add_f32 %0, %1, %2" : "=v"(r) : "v"(a), "v"(b)); return r; }
struct cplx { float re, im; };
DI cplx cmul(cplx a, cplx b) {
  cplx r;
  asm("v_mul_f32 %0, %2, %4\n\tv_mul_f32 %1, %2, %5\n\tv_fma_f32 %0, -%3, %5, %0\n\tv_fma_f32 %1, %3, %4, %1"
      : "=&v"(r.re), "=&v"(r.im) : "v"(a.re), "v"(a.im), "v"(b.re), "v"(b.im));
  return r;
}

constexpr int K1_LDS = 81920;
constexpr int KG_ROW = 272;
constexpr int KG_W = 32 * KG_ROW;
constexpr int KG_KC = 8 * KG_W;
constexpr int QCH = 24576;

__global__ void __launch_bounds__(512, 4)
k1_prep(const u16* __restrict__ x, const u16* __restrict__ lar, const u16* __restrict__ aim,
        const u16* __restrict__ cre, const u16* __restrict__ cim, const u16* __restrict__ logdt,
        const u16* __restrict__ wq, const u16* __restrict__ wk, const u16* __restrict__ wv,
        const u16* __restrict__ gatew, const u16* __restrict__ wo, char* __restrict__ ws) {
  __shared__ __attribute__((aligned(16))) char smem[K1_LDS];
  const int tid = threadIdx.x, lane = tid & 63, w = __builtin_amdgcn_readfirstlane(tid >> 6);
  const int r32 = lane & 31, h = lane >> 5;
  const int b = blockIdx.x;
  if (b < 288) {
    const int ch = w >> 2, half = w & 1, tp = (w >> 1) & 1, n = 64 * half + lane;
    const int d0 = 2 * b, d = d0 + ch;
    const float ldt = bf2f(logdt[d]), la = bf2f(lar[d * NMODE + n]), aimv = bf2f(aim[d * NMODE + n]);
    const float cr0 = bf2f(cre[d * NMODE + n]), ci0 = bf2f(cim[d * NMODE + n]);
    const float cr1 = bf2f(cre[d * NMODE + (n ^ 64)]), ci1 = bf2f(cim[d * NMODE + (n ^ 64)]);
    char* Vt = smem + w * KG_W;
    char* Wt = Vt + 16 * KG_ROW;
    const float dt = rbf(expf(ldt));
    const float ar = -rbf(expf(la)) * dt, ai = aimv * dt;
    float cm = fmaxf(fmaxf(fabsf(cr0), fabsf(ci0)), fmaxf(fabsf(cr1), fabsf(ci1))) * dt;
#pragma unroll
    for (int o = 32; o >= 1; o >>= 1) cm = fmaxf(cm, __shfl_xor(cm, o));
    int ex = 0;
    if (cm > 0.f && cm < 3.0e38f) ex = (int)((__float_as_uint(cm) >> 23) & 255u) - 127;
    ex = max(-120, min(120, ex));
    const float sc = __uint_as_float((unsigned)(127 - ex) << 23), isc2 = __uint_as_float((unsigned)(127 + ex + 1) << 23);
    const cplx cc = {cr0 * dt * sc, ci0 * dt * sc};
    cplx zp[9];
#pragma unroll
    for (int i = 0; i < 9; i++) {
      const float t = (float)(1 << (2 * tp + i));
      const float mag = __builtin_amdgcn_exp2f((ar * t) * 1.4426950408889634f);
      const float rev = __builtin_amdgcn_fractf((ai * t) * 0.15915494309189535f);
      zp[i] = cplx{mag * __builtin_amdgcn_cosf(rev), mag * __builtin_amdgcn_sinf(rev)};
    }
    float* Kc = (float*)(smem + KG_KC) + (2 * ch + half) * 648;
#pragma unroll
    for (int tl = 0; tl < 2; tl++) {
      const int ti = 2 * tp + tl;
      const int nrows = ti == 0 ? 16 : (ti == 3 ? 9 : 8), toff = ti ? 128 + 128 * ti : 0;
      cplx v[4];
      v[0] = cplx{1.f, 0.f};
      v[1] = zp[tl];
      v[2] = zp[tl + 1];
      v[3] = cmul(v[2], zp[tl]);
#pragma unroll
      for (int g = 0; g < 4; g++) {
#pragma unroll
        for (int i = 0; i < 4; i++) {
          *(unsigned*)(Vt + (4 * g + i) * KG_ROW + lane * 4) = pk_f16(v[i].re, v[i].im);
          if (g < 3) v[i] = (g == 0 && i == 0) ? zp[tl + 2] : cmul(v[i], zp[tl + 2]);
        }
      }
      cplx u[4];
      u[0] = cmul(cc, zp[tl + 7]);
      if (tl == 0 && tp == 0) u[0] = cc;
      u[1] = cmul(u[0], zp[tl + 4]);
      u[2] = cmul(u[0], zp[tl + 5]);
      u[3] = cmul(u[2], zp[tl + 4]);
#pragma unroll
      for (int g = 0; g < 4; g++) {
        if (g < 2 || (g == 2 && nrows > 8) || nrows == 16) {
#pragma unroll
          for (int i = 0; i < 4; i++) {
            *(unsigned*)(Wt + (4 * g + i) * KG_ROW + lane * 4) = pk_f16(u[i].re, -u[i].im);
            if (g < 3) u[i] = cmul(u[i], zp[tl + 6]);
          }
        } else {
#pragma unroll
          for (int i = 0; i < 4; i++) *(unsigned*)(Wt + (4 * g + i) * KG_ROW + lane * 4) = 0u;
        }
      }
      wait_lgkm0();
      const int off = (lane & 15) * KG_ROW + (lane >> 4) * 16;
      f32x4 acc = {0.f, 0.f, 0.f, 0.f};
      f16x8 af[4], bf[4];
#pragma unroll
      for (int st = 0; st < 4; st++) {
        af[st] = *(const f16x8*)(Wt + off + st * 64);
        bf[st] = *(const f16x8*)(Vt + off + st * 64);
      }
      wait_lgkm0();
#pragma unroll
      for (int st = 0; st < 4; st++) acc = __builtin_amdgcn_mfma_f32_16x16x32_f16(af[st], bf[st], acc, 0, 0, 0);
#pragma unroll
      for (int rg = 0; rg < 4; rg++) {
        const int a = 4 * (lane >> 4) + rg, idx = toff + 16 * a + (lane & 15);
        if (a < nrows && idx <= 640) Kc[idx] = acc[rg] * isc2;
      }
    }
    lds_barrier();
    const int tt = tid & 255;
    const float* K0 = (const float*)(smem + KG_KC) + 2 * ch * 648;
    float kv[8];
#pragma unroll
    for (int e = 0; e < 8; e++) {
      const int j = 2047 - (8 * tt + e);
      const unsigned ub = __float_as_uint((float)j);
      const int bits = (int)((ub + 0x7fffu + ((ub >> 16) & 1u)) >> 16);
      const int idx = j <= 256 ? j : 256 + bits - 0x4380;
      kv[e] = sadd(K0[idx], K0[648 + idx]);
    }
    st16((u16*)(ws + WS_KR) + (size_t)d * YLD + 8 * tt,
         u32x4{pk_bf16(kv[0], kv[1]), pk_bf16(kv[2], kv[3]), pk_bf16(kv[4], kv[5]), pk_bf16(kv[6], kv[7])});
  } else if (b < 432) {
    __builtin_amdgcn_s_setprio(2);
    const int bq = b - 288, rb = 2 * (bq & 7) + (bq >> 3) / 9, ct = (bq >> 3) % 9, n0 = ct * 64;
    const u16* A = x + (size_t)rb * 128 * DM;
    const u16* W = wq + n0;
    const int G = lane >> 4, i16 = lane & 15;
    auto src = [&](int p) -> const u16* {
      const int row = 8 * (p & 15) + (lane >> 3);
      if (p < 16) return A + (size_t)row * DM + (((lane & 7) ^ ((row >> 1) & 7)) * 8);
      return W + (size_t)row * DM + (((lane & 7) ^ (((row >> 1) & 1) << 2)) * 8);
    };
    {
      const u16 *s0 = src(w), *s1 = src(w + 8), *s2 = src(w + 16);
#pragma unroll
      for (int kc = 0; kc < 3; kc++) {
        glds16(s0 + kc * 64, smem + kc * QCH + w * 1024);
        glds16(s1 + kc * 64, smem + kc * QCH + (w + 8) * 1024);
        glds16(s2 + (size_t)kc * 64 * DM, smem + kc * QCH + (w + 16) * 1024);
      }
    }
    if (w >= 4) {
      const int L = w - 4;
      const u16* sp[6];
#pragma unroll
      for (int j = 0; j < 6; j++) sp[j] = src(L + 4 * j);
#pragma unroll
      for (int c = 0; c < 9; c++) {
        if (c == 0) wait_vm<6>();
        else if (c == 8) wait_vm<0>();
        else if (c == 1) { if (ct == 0) wait_vm<7>(); else wait_vm<3>(); }
        else if (c == ct + 1 || c == ct + 2) wait_vm<10>();
        else wait_vm<6>();
        lds_barrier();
        if (c >= 1 && c + 2 < 9) {
          char* buf = smem + ((c + 2) % 3) * QCH + L * 1024;
#pragma unroll
          for (int j = 0; j < 6; j++) glds16(sp[j] + (size_t)(c + 2) * (j < 4 ? 64 : 64 * DM), buf + j * 4096);
        }
        if (c == ct) {
          const char* buf = smem + (c % 3) * QCH;
          const int l0 = 32 * L + 8 * G, ra = l0 + (i16 >> 2), rbw = ra + 4;
#pragma unroll
          for (int cj = 0; cj < 4; cj++) {
            const int c16 = 2 * cj + ((i16 >> 1) & 1);
            const s16x4 lo = tr_read(buf + ra * 128 + ((c16 ^ ((ra >> 1) & 7)) * 16) + (i16 & 1) * 8);
            const s16x4 hi = tr_read(buf + rbw * 128 + ((c16 ^ ((rbw >> 1) & 7)) * 16) + (i16 & 1) * 8);
            st16((u16*)(ws + WS_UT) + (size_t)(64 * c + 16 * cj + i16) * YLD + 128 * rb + l0,
                 __builtin_bit_cast(u32x4, __builtin_shufflevector(lo, hi, 0, 1, 2, 3, 4, 5, 6, 7)));
          }
        }
      }
    } else {
      const int arow = 32 * w + r32, aoff = arow * 128, asw = (arow >> 1) & 7;
      const int q4 = i16 >> 2, p4 = i16 & 3;
      const int bo0 = 16384 + (8 * (G >> 1) + q4) * 128 + (((2 * (G & 1) + (p4 >> 1)) ^ (((q4 >> 1) & 1) << 2)) * 16) + (p4 & 1) * 8;
      const int bo1 = bo0 ^ 64;
      f32x16 acc0 = zero16(), acc1 = zero16();
#pragma unroll
      for (int kc = 0; kc < 9; kc++) {
        if (kc == 0) wait_vm<6>();
        else if (kc == 1) wait_vm<3>();
        else if (kc == 2) wait_vm<0>();
        lds_barrier();
        const char* buf = smem + (kc % 3) * QCH;
        bf16x8 fa[4], fb0[4], fb1[4];
#pragma unroll
        for (int s = 0; s < 4; s++) {
          fa[s] = *(const bf16x8*)(buf + aoff + (((2 * s + h) ^ asw) * 16));
          s16x4 lo = tr_read(buf + bo0 + 2048 * s), hi = tr_read(buf + bo0 + 2048 * s + 512);
          fb0[s] = __builtin_shufflevector(lo, hi, 0, 1, 2, 3, 4, 5, 6, 7);
          lo = tr_read(buf + bo1 + 2048 * s);
          hi = tr_read(buf + bo1 + 2048 * s + 512);
          fb1[s] = __builtin_shufflevector(lo, hi, 0, 1, 2, 3, 4, 5, 6, 7);
        }
#pragma unroll
        for (int s = 0; s < 4; s++) {
          acc0 = MFMA32(fa[s], fb0[s], acc0);
          acc1 = MFMA32(fa[s], fb1[s], acc1);
        }
      }
      const float c = 0.23570226039551584f * 1.4426950408889634f;
      u16* Qs = (u16*)(ws + WS_Q) + (size_t)(128 * rb + 32 * w + 4 * h) * DM + n0 + r32;
#pragma unroll
      for (int i = 0; i < 16; i++) {
        st2(Qs + ((i & 3) + 8 * (i >> 2)) * DM, pk_bf16(acc0[i] * c, 0.f));
        st2(Qs + ((i & 3) + 8 * (i >> 2)) * DM + 32, pk_bf16(acc1[i] * c, 0.f));
      }
    }
  } else if (b < 496) {
    const int T = b - 432;
    const u16* xa = x + (size_t)T * 32 * DM;
    for (int p = w; p < 32; p += 8) {
      const int row = 8 * (p & 3) + (lane >> 3);
      glds16(xa + (size_t)row * DM + (p >> 2) * 64 + (((lane & 7) ^ ((row >> 1) & 7)) * 8), smem + p * 1024);
    }
    char* bres = smem + 32768;
    for (int k = tid; k < DM; k += 512) {
      const u32x4* sk = (const u32x4*)(wk + k * DHEAD);
      const u32x4* sv = (const u32x4*)(wv + k * DHEAD);
      const u32x4 k0 = sk[0], k1 = sk[1], v0 = sv[0], v1 = sv[1];
      const unsigned k2 = ((const unsigned*)sk)[8], v2 = ((const unsigned*)sv)[8], gw = gatew[k];
      u32x4* dst = (u32x4*)(bres + k * 80);
      dst[0] = k0;
      dst[1] = k1;
      dst[2] = u32x4{k2, gw, v0[0], v0[1]};
      dst[3] = u32x4{v0[2], v0[3], v1[0], v1[1]};
      dst[4] = u32x4{v1[2], v1[3], v2, 0u};
    }
    if (tid < 2) *(u32x4*)(bres + DM * 80 + tid * 16) = u32x4{0u, 0u, 0u, 0u};
    const int isv = w & 1, kq = w >> 1;
    bf16x8 atail[4];
    if (kq == 3) {
#pragma unroll
      for (int s = 0; s < 4; s++) atail[s] = *(const bf16x8*)(xa + (size_t)r32 * DM + 512 + 16 * s + 8 * h);
    }
    wait_vm<0>();
    __syncthreads();
    f32x16 acc = zero16();
    {
      const int G = lane >> 4, i16 = lane & 15;
      const char* bb = bres + (144 * kq + 8 * (G >> 1) + (i16 >> 2)) * 80 + (20 * isv + 16 * (G & 1) + 4 * (i16 & 3)) * 2;
      const char* ab = smem + r32 * 128;
      const int asw = (r32 >> 1) & 7;
#pragma unroll
      for (int i = 0; i < 9; i++) {
        const int st = 9 * kq + i;
        bf16x8 av = *(const bf16x8*)(ab + ((st >> 2) & 7) * 4096 + (((2 * (st & 3) + h) ^ asw) * 16));
        if (i >= 5 && kq == 3) av = atail[i - 5];
        s16x4 lo = tr_read(bb + 16 * i * 80), hi = tr_read(bb + (16 * i + 4) * 80);
        const bf16x8 wv_ = __builtin_shufflevector(lo, hi, 0, 1, 2, 3, 4, 5, 6, 7);
        acc = isv ? MFMA32(av, wv_, acc) : MFMA32(wv_, av, acc);
      }
    }
    lds_barrier();
    float* red = (float*)smem;
    if (w >= 2) {
#pragma unroll
      for (int g = 0; g < 4; g++)
        *(f32x4*)(red + (((w - 2) * 4 + g) * 64 + lane) * 4) = f32x4{acc[4 * g], acc[4 * g + 1], acc[4 * g + 2], acc[4 * g + 3]};
    }
    lds_barrier();
    if (w < 2) {
#pragma unroll
      for (int pq = 0; pq < 3; pq++) {
#pragma unroll
        for (int g = 0; g < 4; g++) {
          const f32x4 pv = *(const f32x4*)(red + (((2 * pq + w) * 4 + g) * 64 + lane) * 4);
#pragma unroll
          for (int e = 0; e < 4; e++) acc[4 * g + e] = sadd(acc[4 * g + e], pv[e]);
        }
      }
      char* blk = ws + WS_KV + (size_t)(T >> 2) * 9216;
      const int wr = T & 3;
      if (w == 0) {
        char* t0 = blk + wr * 1024;
        st8(t0 + r32 * 16 + 8 * h, u32x2{pk_bf16(acc[0], acc[1]), pk_bf16(acc[2], acc[3])});
        st8(t0 + (32 + r32) * 16 + 8 * h, u32x2{pk_bf16(acc[4], acc[5]), pk_bf16(acc[6], acc[7])});
        if (!h) {
          st4(blk + 4096 + wr * 128 + r32 * 4, pk_bf16(acc[8], acc[9]));
          const float z = rbf(acc[10]);
          st4((float*)(ws + WS_G) + 32 * T + r32, __float_as_uint(rbf(1.0f / (1.0f + expf(-z)))));
        }
      } else if (r32 < DHEAD) {
        char* t0 = blk + 4608 + wr * 1152 + h * 288 + r32 * 16;
        st16(t0, u32x4{pk_bf16(acc[0], acc[1]), pk_bf16(acc[2], acc[3]), pk_bf16(acc[4], acc[5]), pk_bf16(acc[6], acc[7])});
        st16(t0 + 576, u32x4{pk_bf16(acc[8], acc[9]), pk_bf16(acc[10], acc[11]), pk_bf16(acc[12], acc[13]), pk_bf16(acc[14], acc[15])});
      }
    }
  } else {
    const int t2 = tid & 127;
    u16* TW = (u16*)(smem + (w >> 1) * 9472);
#pragma unroll
    for (int rnd = 0; rnd < 2; rnd++) {
      const int ul = 4 * rnd + (w >> 1), unit = 6 * (b - 496) + ul, ks = unit >> 1, c0 = (unit & 1) * 288;
      if (ul < 6) {
#pragma unroll
        for (int j = 0; j < 5; j++) {
          const int e = t2 + 128 * j;
          if (e < 576)
            *(bf16x8*)(TW + (e / 36) * 296 + (e % 36) * 8) = *(const bf16x8*)(wo + (size_t)(16 * ks + e / 36) * DM + c0 + (e % 36) * 8);
        }
      }
      lds_barrier();
      if (ul < 6) {
#pragma unroll
        for (int j = 0; j < 5; j++) {
          const int e = t2 + 128 * j;
          if (e < 576) {
            const int ctl = e / 144, q = e % 144;
            const int hh = q < 128 ? (q >> 5) & 1 : (q >> 3) & 1;
            const int col = 72 * ctl + (q < 128 ? 32 * (q >> 6) + (q & 31) : 64 + (q & 7));
            unsigned o4[4];
#pragma unroll
            for (int i = 0; i < 4; i++)
              o4[i] = (unsigned)TW[(8 * hh + 2 * i) * 296 + col] | ((unsigned)TW[(8 * hh + 2 * i + 1) * 296 + col] << 16);
            st16(ws + WS_WP + ((size_t)((unit & 1) * 4 + ctl) * 36 + ks) * 2304 + q * 16, u32x4{o4[0], o4[1], o4[2], o4[3]});
          }
        }
      }
      lds_barrier();
    }
  }
}

constexpr int AT_C = 73728, CV_BASE = AT_C + 4096, CV_SLOT = 24576, CV_RED = CV_BASE + 3 * CV_SLOT, K2_LDS = CV_RED + 12288;
constexpr int CV_COPY = 4 * 4160;

DI u32x4 exp_pack(const f32x16& s, int o) {
  u32x4 p;
#pragma unroll
  for (int i = 0; i < 4; i++)
    p[i] = pk_bf16(__builtin_amdgcn_exp2f(s[o + 2 * i]), __builtin_amdgcn_exp2f(s[o + 2 * i + 1]));
  return p;
}
DI float tile_max(const f32x16& s) {
  float m = s[0];
#pragma unroll
  for (int i = 1; i < 16; i++) m = fmaxf(m, s[i]);
  return m;
}
DI void set_shift(u32x4& q1, float m, int h) {
  const float nm = -m, hi = rbf(nm), r1 = nm - hi, mid = rbf(r1), lo = rbf(r1 - mid);
  if (!h) {
    q1[1] = pk_bf16(hi, mid);
    q1[2] = pk_bf16(lo, 0.f);
  }
}

__global__ void __launch_bounds__(512, 2)
k2_attn_conv(char* __restrict__ ws) {
  __shared__ __attribute__((aligned(16))) char smem[K2_LDS];
  const int tid = threadIdx.x, lane = tid & 63, w = __builtin_amdgcn_readfirstlane(tid >> 6);
  const int r32 = lane & 31, h = lane >> 5;
  const int g = blockIdx.x, q = g * 8 + w;
  const int tt8 = tid & 255;
  const int dst0 = 2 * g + (tid >> 8), dst2 = 512 + (g >> 1);
  const u16* qp = (const u16*)(ws + WS_Q) + (size_t)q * DM + r32 * DHEAD;
  u32x4 q0;
  unsigned q16;
  asm volatile("global_load_dwordx4 %0, %2, off\n\tglobal_load_dword %1, %3, off"
               : "=&v"(q0), "=&v"(q16) : "v"(qp + 8 * h), "v"(qp + 16) : "memory");
  const char* src = ws + WS_KV + w * 1024 + lane * 16;
  auto issue = [&](int j) { glds16(src + j * 8192, smem + w * 1024 + (j >= 9 ? j - 9 : j) * 8192); };
  issue(0);
  issue(1);
  issue(2);
  const bool two = g < 128 && w < 4;
  u32x4 ka[2], uv[2];
  u32x2 kb[2];
#pragma unroll
  for (int rd = 0; rd < 2; rd++) {
    const int d = (rd && two) ? dst2 : dst0;
    asm volatile("global_load_dwordx4 %0, %3, off\n\tglobal_load_dwordx2 %1, %3, off offset:16\n\tglobal_load_dwordx4 %2, %4, off"
                 : "=&v"(ka[rd]), "=&v"(kb[rd]), "=&v"(uv[rd])
                 : "v"((const u16*)(ws + WS_KR) + (size_t)d * YLD + 8 * tt8), "v"((const u16*)(ws + WS_UT) + (size_t)d * YLD + 8 * tt8)
                 : "memory");
  }
  if (tid < 8) {
    *(u32x4*)(smem + AT_C + tid * 576) = u32x4{0x3f803f80u, 0x3f803f80u, 0x3f803f80u, 0x3f803f80u};
    *(u32x4*)(smem + AT_C + tid * 576 + 16) = u32x4{0u, 0u, 0u, 0u};
    if (tid < 4) *(unsigned*)(smem + AT_C + 32 + tid * 128) = 0u;
  }
  asm volatile("s_waitcnt vmcnt(6) lgkmcnt(0)" : "+v"(q0), "+v"(q16)::"memory");
  barrier_raw();
  auto stage_conv = [&] {
    asm volatile("s_waitcnt vmcnt(2)"
                 : "+v"(ka[0]), "+v"(kb[0]), "+v"(uv[0]), "+v"(ka[1]), "+v"(kb[1]), "+v"(uv[1])::"memory");
#pragma unroll
    for (int rd = 0; rd < 2; rd++) {
      if (rd == 0 || two) {
        char* base = smem + CV_BASE + (rd ? 2 : tid >> 8) * CV_SLOT;
        if (tt8 == 255) kb[rd] = u32x2{0u, 0u};
        const unsigned dw[6] = {ka[rd][0], ka[rd][1], ka[rd][2], ka[rd][3], kb[rd][0], kb[rd][1]};
#pragma unroll
        for (int sft = 0; sft < 4; sft++) {
          u32x4 v;
#pragma unroll
          for (int e = 0; e < 4; e++)
            v[e] = (sft & 1) ? __builtin_amdgcn_alignbit(dw[(sft >> 1) + e + 1], dw[(sft >> 1) + e], 16) : dw[(sft >> 1) + e];
          char* cpy = base + (3 - sft) * 4160;
          *(u32x4*)(cpy + tt8 * 16) = v;
          if (tt8 < 4) *(u32x4*)(cpy + 4096 + tt8 * 16) = u32x4{0u, 0u, 0u, 0u};
        }
        if (tt8 < 155) *(u32x4*)(base + CV_COPY + tt8 * 16) = u32x4{0u, 0u, 0u, 0u};
        *(u32x4*)(base + CV_COPY + (31 + (tt8 >> 2)) * 80 + (tt8 & 3) * 16) = uv[rd];
      }
    }
  };
  const int cslot = w < 3 ? 0 : (w < 6 ? 1 : (g < 128 ? 2 : 0));
  const int seg = w < 6 ? w % 3 : ((g & 1) ? 0 : w - 5);
  const bool cactive = w < 6 || (g < 128 && (w == 6 || !(g & 1)));
  const int kend = seg == 2 ? 63 : 31;
  const int cbp = CV_BASE + cslot * CV_SLOT + (r32 & 3) * 4160 + (511 + 2 * h - (r32 >> 2)) * 8 - kend * 64;
  const int cap = CV_BASE + cslot * CV_SLOT + CV_COPY + (cactive ? (r32 + 31 + (seg ? 32 : 0) - kend) * 80 + h * 16 : 0);
  auto ldcb = [&](int j, int hf) {
    const char* tq = smem + cbp + (31 - j) * 64 + 32 * hf;
    const u32x2 ta = *(const u32x2*)(tq), tb = *(const u32x2*)(tq + 8);
    return asbf(u32x4{ta[0], ta[1], tb[0], tb[1]});
  };
  auto ldca = [&](int j, int hf) { return *(const bf16x8*)(smem + cap + (31 - j) * 80 + 32 * hf); };
  float* red = (float*)(smem + CV_RED + cslot * 4096);

  u32x4 q1 = {h ? 0u : q16, 0u, 0u, 0u};
  const unsigned kc1 = h ? 0u : 0x3f803f80u, kc2 = h ? 0u : 0x00003f80u;
  const int k1step = h ? 0 : 9216, vstep = r32 < DHEAD ? 9216 : 0;
  const int k0base = lane * 16;
  const int k1base = h ? AT_C + 32 : 4096 + r32 * 4;
  const int vbase = r32 < DHEAD ? 4608 + h * 288 + r32 * 16 : (r32 == DHEAD ? AT_C : AT_C + 16);
  auto ldk0 = [&](int a, int t) { return *(const bf16x8*)(smem + a + t * 1024); };
  auto ldk1 = [&](int a, int t) { return asbf(u32x4{*(const unsigned*)(smem + a + t * 128), kc1, kc2, 0u}); };
  auto ldv = [&](int a, int t, int s) { return *(const bf16x8*)(smem + a + t * 1152 + s * 576); };

  bf16x8 k0 = ldk0(k0base, 0), k1 = ldk1(k1base, 0);
  {
    f32x16 s = MFMA32(k0, asbf(q0), zero16());
    s = MFMA32(k1, asbf(q1), s);
    float m = tile_max(s);
    m = fmaxf(m, __shfl_xor(m, 32));
    set_shift(q1, m, h);
  }
  f32x16 o = zero16(), cacc = zero16();
  f32x16 s = MFMA32(k0, asbf(q0), zero16());
  s = MFMA32(k1, asbf(q1), s);
  k0 = ldk0(k0base, 1);
  k1 = ldk1(k1base, 1);
  bf16x8 v0 = ldv(vbase, 0, 0), v1 = ldv(vbase, 0, 1);
  bf16x8 ca0 = k0, cb0 = k0, ca1 = k0, cb1 = k0;
  int ak0 = k0base, ak1 = k1base, av = vbase;
#pragma unroll
  for (int c = 0; c < 16; c++) {
    const int ns = c == 15 ? 7 : ((c + 1) & 7);
    const int nk0 = k0base + ns * 9216, nk1 = k1base + ns * k1step, nv = vbase + ns * vstep;
#pragma unroll
    for (int tt = 0; tt < 4; tt++) {
      const int T = 4 * c + tt;
      const bool cv0 = T >= 8 && T < 56, cv1 = T >= 8 && T < 24;
      u32x4 p0, p1;
      p0[0] = pk_bf16(__builtin_amdgcn_exp2f(s[0]), __builtin_amdgcn_exp2f(s[1]));
      const float e2 = __builtin_amdgcn_exp2f(s[2]);
      SCHED_FENCE();
      f32x16 sn = MFMA32(k0, asbf(q0), zero16());
      SCHED_FENCE();
      p0[1] = pk_bf16(e2, __builtin_amdgcn_exp2f(s[3]));
      const float e4 = __builtin_amdgcn_exp2f(s[4]), e5 = __builtin_amdgcn_exp2f(s[5]);
      SCHED_FENCE();
      if (cv0) cacc = MFMA32(ca0, cb0, cacc);
      SCHED_FENCE();
      p0[2] = pk_bf16(e4, e5);
      p0[3] = pk_bf16(__builtin_amdgcn_exp2f(s[6]), __builtin_amdgcn_exp2f(s[7]));
      SCHED_FENCE();
      o = MFMA32(v0, asbf(p0), o);
      SCHED_FENCE();
      p1[0] = pk_bf16(__builtin_amdgcn_exp2f(s[8]), __builtin_amdgcn_exp2f(s[9]));
      const float e10 = __builtin_amdgcn_exp2f(s[10]);
      SCHED_FENCE();
      if (cv1) cacc = MFMA32(ca1, cb1, cacc);
      SCHED_FENCE();
      p1[1] = pk_bf16(e10, __builtin_amdgcn_exp2f(s[11]));
      const float e12 = __builtin_amdgcn_exp2f(s[12]);
      SCHED_FENCE();
      sn = MFMA32(k1, asbf(q1), sn);
      if (T == 2) stage_conv();
      if (T == 6) {
        wait_vm<2>();
        lds_barrier();
      } else if (T == 22 || T == 30 || T == 38 || T == 54) {
        wait_vm<0>();
        lds_barrier();
      }
      if (T < 6) issue(3 + T);
      if (T >= 22 && T < 27) issue(T - 13);
      if (T == 30 || T == 31) issue(T - 16);
      if (T == 38 || T == 39) issue(T - 22);
      if (T == 57 && cactive && seg == 2) {
#pragma unroll
        for (int gi = 0; gi < 4; gi++)
          *(f32x4*)(red + (gi * 64 + lane) * 4) = f32x4{cacc[4 * gi], cacc[4 * gi + 1], cacc[4 * gi + 2], cacc[4 * gi + 3]};
      }
      if (T == 59) lds_barrier();
      if (T == 60 && cactive && seg != 2) {
        if (seg == 1) {
#pragma unroll
          for (int gi = 0; gi < 4; gi++) {
            const f32x4 pv = *(const f32x4*)(red + (gi * 64 + lane) * 4);
#pragma unroll
            for (int e = 0; e < 4; e++) cacc[4 * gi + e] = sadd(cacc[4 * gi + e], pv[e]);
          }
        }
        const int d = cslot == 2 ? dst2 : 2 * g + cslot;
        u16* yT = (u16*)(ws + WS_YT) + (size_t)d * 64;
#pragma unroll
        for (int i = 0; i < 16; i++) {
          const int I = 32 * seg + (i & 3) + 8 * (i >> 2) + 4 * h;
          st2(yT + (size_t)(I >> 1) * (DM * 64) + (I & 1) * 32 + r32, pk_bf16(cacc[i], 0.f));
        }
      }
      k0 = tt < 2 ? ldk0(ak0, tt + 2) : ldk0(nk0, tt - 2);
      k1 = tt < 2 ? ldk1(ak1, tt + 2) : ldk1(nk1, tt - 2);
      v0 = tt < 3 ? ldv(av, tt + 1, 0) : ldv(nv, 0, 0);
      const bf16x8 vn1 = tt < 3 ? ldv(av, tt + 1, 1) : ldv(nv, 0, 1);
      if (T >= 7 && T < 23) {
        ca0 = ldca(T - 7, 0);
        cb0 = ldcb(T - 7, 0);
        ca1 = ldca(T - 7, 1);
        cb1 = ldcb(T - 7, 1);
      } else if (T >= 23 && T < 55) {
        ca0 = ldca((T + 9) >> 1, (T + 9) & 1);
        cb0 = ldcb((T + 9) >> 1, (T + 9) & 1);
      }
      SCHED_FENCE();
      p1[2] = pk_bf16(e12, __builtin_amdgcn_exp2f(s[13]));
      p1[3] = pk_bf16(__builtin_amdgcn_exp2f(s[14]), __builtin_amdgcn_exp2f(s[15]));
      SCHED_FENCE();
      o = MFMA32(v1, asbf(p1), o);
      SCHED_FENCE();
      v1 = vn1;
      s = sn;
    }
    ak0 = nk0;
    ak1 = nk1;
    av = nv;
  }
  bool bad = false;
#pragma unroll
  for (int i = 0; i < 12; i++) bad |= !(fabsf(o[i]) < 1.0e30f);
  if (__any(bad)) {
    const char* gk = ws + WS_KV;
    auto gk0 = [&](int t) { return *(const bf16x8*)(gk + (t >> 2) * 9216 + (t & 3) * 1024 + lane * 16); };
    auto gk1 = [&](int t) {
      const unsigned v = *(const unsigned*)(gk + (t >> 2) * 9216 + 4096 + (t & 3) * 128 + r32 * 4);
      return asbf(u32x4{h ? 0u : v, kc1, kc2, 0u});
    };
    auto gv = [&](int t, int st) {
      const int dd = r32 < DHEAD ? r32 : 0;
      u32x4 v = *(const u32x4*)(gk + (t >> 2) * 9216 + 4608 + (t & 3) * 1152 + st * 576 + h * 288 + dd * 16);
      const unsigned cv = r32 == DHEAD ? 0x3f803f80u : 0u;
      if (r32 >= DHEAD) v = u32x4{cv, cv, cv, cv};
      return asbf(v);
    };
    q1[1] = 0u;
    q1[2] = 0u;
    float m = -__builtin_inff();
    for (int t = 0; t < 64; t++) {
      f32x16 st = MFMA32(gk0(t), asbf(q0), zero16());
      st = MFMA32(gk1(t), asbf(q1), st);
      m = fmaxf(m, tile_max(st));
    }
    m = fmaxf(m, __shfl_xor(m, 32));
    set_shift(q1, m, h);
    o = zero16();
    for (int t = 0; t < 64; t++) {
      f32x16 st = MFMA32(gk0(t), asbf(q0), zero16());
      st = MFMA32(gk1(t), asbf(q1), st);
      o = MFMA32(gv(t, 0), asbf(exp_pack(st, 0)), o);
      o = MFMA32(gv(t, 1), asbf(exp_pack(st, 8)), o);
    }
  }
  const float l = __shfl(o[10], r32);
  const float il = 1.0f / l;
  unsigned* op = (unsigned*)((u16*)(ws + WS_O) + (size_t)q * DM + r32 * DHEAD);
  st4(op + 2 * h, pk_bf16(o[0] * il, o[1] * il));
  st4(op + 2 * h + 1, pk_bf16(o[2] * il, o[3] * il));
  st4(op + 4 + 2 * h, pk_bf16(o[4] * il, o[5] * il));
  st4(op + 4 + 2 * h + 1, pk_bf16(o[6] * il, o[7] * il));
  if (!h) st4(op + 8, pk_bf16(o[8] * il, o[9] * il));
}

constexpr int GCH = 17408, K3_G = 9 * GCH, K3_LDS = K3_G + 512;

__global__ void __launch_bounds__(768, 3)
k3_out(const u16* __restrict__ x, const u16* __restrict__ dskip, const char* __restrict__ ws, u16* __restrict__ out) {
  __shared__ __attribute__((aligned(16))) char smem[K3_LDS];
  const int tid = threadIdx.x, lane = tid & 63, w = __builtin_amdgcn_readfirstlane(tid >> 6);
  const int r32 = lane & 31, h = lane >> 5;
  const int rb = 4 * (blockIdx.x & 7) + ((blockIdx.x >> 3) & 3), ct = blockIdx.x >> 5, n0 = ct * 72;
  if (w >= 6) {
    const int L = w - 6;
    const u16* A = (const u16*)(ws + WS_O) + (size_t)rb * 64 * DM;
    const char* W = ws + WS_WP + (size_t)ct * 82944;
    if (L == 5 && lane < 16) glds16(ws + WS_G + (size_t)rb * 256 + lane * 16, smem + K3_G);
    if (L == 5 && lane < 9) glds16(dskip + n0 + lane * 8, smem + K3_G + 256);
    const char* src[3];
    int step[3];
#pragma unroll
    for (int j = 0; j < 3; j++) {
      const int pp = L + 6 * j > 16 ? L : L + 6 * j;
      if (pp < 8) {
        const int row = 8 * pp + (lane >> 3);
        src[j] = (const char*)(A + (size_t)row * DM + (((lane & 7) ^ ((row >> 1) & 7)) * 8));
        step[j] = 128;
      } else {
        src[j] = W + (pp - 8) * 1024 + lane * 16;
        step[j] = 9216;
      }
    }
    auto issue = [&](int kc) {
#pragma unroll
      for (int j = 0; j < 3; j++)
        glds16(src[j] + kc * step[j], smem + kc * GCH + (L + 6 * j > 16 ? L : L + 6 * j) * 1024);
    };
#pragma unroll
    for (int kc = 0; kc < 4; kc++) issue(kc);
    wait_vm<6>();
    barrier_raw();
    issue(4);
    issue(5);
    wait_vm<6>();
    barrier_raw();
    issue(6);
    issue(7);
    issue(8);
    wait_vm<9>();
    barrier_raw();
#pragma unroll
    for (int j = 0; j < 3; j++) {
      const int p = L + 6 * j;
      if (p < 9) {
        const int dd = 8 * p + (lane >> 3);
        glds16((const u16*)(ws + WS_YT) + ((size_t)rb * DM + n0 + dd) * 64 + (lane & 7) * 8, smem + p * 1024);
      } else {
        const int P = 64 * (p - 9) + lane, l = P / 9;
        glds16(x + (size_t)(64 * rb + l) * DM + n0 + (P - 9 * l) * 8, smem + GCH + (p - 9) * 1024);
      }
    }
    wait_vm<3>();
    barrier_raw();
    wait_vm<0>();
  } else {
    const int wr = w / 3, wc = w % 3;
    const int arow = 32 * wr + r32, aoff = arow * 128, asw = (arow >> 1) & 7;
    const int boff = 8192 + wc * 1024 + (wc == 2 ? ((lane & 7) + 8 * h) * 16 : lane * 16);
    bf16x8 fa[2][4], fb[2][4];
    auto ld = [&](int kc) {
      const char* buf = smem + kc * GCH;
#pragma unroll
      for (int s = 0; s < 4; s++) {
        fa[kc & 1][s] = *(const bf16x8*)(buf + aoff + (((2 * s + h) ^ asw) * 16));
        fb[kc & 1][s] = *(const bf16x8*)(buf + boff + 2304 * s);
      }
    };
    f32x16 acc = zero16();
#pragma unroll
    for (int kc = 0; kc < 9; kc++) {
      if (kc == 0 || kc == 2 || kc == 4 || kc == 6) {
        lds_barrier();
        ld(kc);
      }
      if (kc == 0 || kc == 2 || kc == 4 || kc == 6 || kc == 7) ld(kc + 1);
#pragma unroll
      for (int s = 0; s < 4; s++) acc = MFMA32(fa[kc & 1][s], fb[kc & 1][s], acc);
    }
    const int dl = 32 * wc + r32;
    if (dl < 72) {
      u16* bt = (u16*)(smem + 2 * GCH) + (32 * wr + 4 * h) * 72 + dl;
#pragma unroll
      for (int i = 0; i < 16; i++) bt[((i & 3) + 8 * (i >> 2)) * 72] = (u16)pk_bf16(acc[i], 0.f);
    }
    wait_lgkm0();
  }
  barrier_raw();
  if (tid < 576) {
    const int l = tid / 9, c8 = (tid - 9 * l) * 8;
    const u32x4 bv = *(const u32x4*)(smem + 2 * GCH + l * 144 + c8 * 2);
    const u32x4 uv = *(const u32x4*)(smem + GCH + l * 144 + c8 * 2);
    const u32x4 dv = *(const u32x4*)(smem + K3_G + 256 + c8 * 2);
    const float gg = *(const float*)(smem + K3_G + l * 4), omg = rbf(1.0f - gg);
    float r[8];
#pragma unroll
    for (int e = 0; e < 8; e++) {
      const float y = bf2f(*(const u16*)(smem + (c8 + e) * 128 + l * 2));
      const float u = (e & 1) ? bfhi(uv[e >> 1]) : bflo(uv[e >> 1]);
      const float ds = (e & 1) ? bfhi(dv[e >> 1]) : bflo(dv[e >> 1]);
      const float bb = (e & 1) ? bfhi(bv[e >> 1]) : bflo(bv[e >> 1]);
      const float a = rbf(sadd(y, rbf(smul(u, ds))));
      r[e] = sadd(rbf(smul(gg, a)), rbf(smul(omg, bb)));
    }
    st16(out + (size_t)(64 * rb + l) * DM + n0 + c8,
         u32x4{pk_bf16(r[0], r[1]), pk_bf16(r[2], r[3]), pk_bf16(r[4], r[5]), pk_bf16(r[6], r[7])});
  }
}

extern "C" void kernel_launch(void* const* d_in, const int* in_sizes, int n_in, void* d_out, int out_size,
                              void* d_ws, size_t ws_size, hipStream_t stream) {
  const u16* x = (const u16*)d_in[0];
  const u16* lar = (const u16*)d_in[1];
  const u16* aim = (const u16*)d_in[2];
  const u16* cre = (const u16*)d_in[3];
  const u16* cim = (const u16*)d_in[4];
  const u16* logdt = (const u16*)d_in[5];
  const u16* dskip = (const u16*)d_in[6];
  const u16* wq = (const u16*)d_in[8];
  const u16* wk = (const u16*)d_in[9];
  const u16* wv = (const u16*)d_in[10];
  const u16* wo = (const u16*)d_in[11];
  const u16* gatew = (const u16*)d_in[12];
  char* ws = (char*)d_ws;
  k1_prep<<<508, 512, 0, stream>>>(x, lar, aim, cre, cim, logdt, wq, wk, wv, gatew, wo, ws);
  k2_attn_conv<<<256, 512, 0, stream>>>(ws);
  k3_out<<<256, 768, 0, stream>>>(x, dskip, ws, (u16*)d_out);
}
```

```cpp
#include <hip/hip_runtime.h>
#include <hip/hip_bf16.h>
#include <stdint.h>

typedef unsigned short u16;
typedef __attribute__((ext_vector_type(8))) short bf16x8;
typedef __attribute__((ext_vector_type(4))) short s16x4;
typedef __attribute__((ext_vector_type(8))) _Float16 f16x8;
typedef __attribute__((ext_vector_type(2))) _Float16 f16x2;
typedef __attribute__((ext_vector_type(2))) __bf16 bf16x2;
typedef __attribute__((ext_vector_type(2))) float f32x2;
typedef __attribute__((ext_vector_type(4))) float f32x4;
typedef __attribute__((ext_vector_type(16))) float f32x16;
typedef __attribute__((ext_vector_type(4))) unsigned u32x4;
typedef __attribute__((ext_vector_type(2))) unsigned u32x2;

#define DI __device__ __forceinline__
#define LDSP(p) ((__attribute__((address_space(3))) void*)(p))
#define GLBP(p) ((const __attribute__((address_space(1))) void*)(p))
#define MFMA32(a, b, c) __builtin_amdgcn_mfma_f32_32x32x16_bf16((a), (b), (c), 0, 0, 0)
#define SCHED_FENCE() __builtin_amdgcn_sched_barrier(0)

constexpr int SEQ = 2048, DM = 576, NMODE = 128, DHEAD = 18;
constexpr int YLD = 2112;

constexpr size_t WS_Q = 0;
constexpr size_t WS_O = WS_Q + 2359296;
constexpr size_t WS_YT = WS_O + 2359296;
constexpr size_t WS_KV = WS_YT + 2359296;
constexpr size_t WS_G = WS_KV + 147456;
constexpr size_t WS_WP = WS_G + 8192;
constexpr size_t WS_UT = WS_WP + 663552;
constexpr size_t WS_KR = WS_UT + 2433024;

DI float bf2f(u16 b) { return __uint_as_float((unsigned)b << 16); }
DI float bflo(unsigned b) { return __uint_as_float(b << 16); }
DI float bfhi(unsigned b) { return __uint_as_float(b & 0xffff0000u); }
DI unsigned pk_bf16(float lo, float hi) {
  f32x2 f = {lo, hi};
  return __builtin_bit_cast(unsigned, __builtin_convertvector(f, bf16x2));
}
DI unsigned pk_f16(float lo, float hi) {
  f32x2 f = {lo, hi};
  return __builtin_bit_cast(unsigned, __builtin_convertvector(f, f16x2));
}
DI float rbf(float x) { return __uint_as_float(pk_bf16(x, 0.f) << 16); }
DI void glds16(const void* g, void* l) { __builtin_amdgcn_global_load_lds(GLBP(g), LDSP(l), 16, 0, 0); }
template <int N>
DI void wait_vm() { asm volatile("s_waitcnt vmcnt(%0)" ::"n"(N) : "memory"); }
DI void wait_lgkm0() { asm volatile("s_waitcnt lgkmcnt(0)" ::: "memory"); }
DI void barrier_raw() { __builtin_amdgcn_s_barrier(); }
DI void lds_barrier() { wait_lgkm0(); barrier_raw(); }
DI s16x4 tr_read(const char* p) {
  return __builtin_amdgcn_ds_read_tr16_b64_v4i16((__attribute__((address_space(3))) s16x4*)(p));
}
DI bf16x8 asbf(u32x4 v) { return __builtin_bit_cast(bf16x8, v); }
DI f32x16 zero16() {
  f32x16 z;
#pragma unroll
  for (int i = 0; i < 16; i++) z[i] = 0.f;
  return z;
}
DI void st16(void* p, u32x4 v) { asm volatile("global_store_dwordx4 %0, %1, off sc1\n\ts_nop 1" ::"v"(p), "v"(v) : "memory"); }
DI void st8(void* p, u32x2 v) { asm volatile("global_store_dwordx2 %0, %1, off sc1" ::"v"(p), "v"(v) : "memory"); }
DI void st4(void* p, unsigned v) { asm volatile("global_store_dword %0, %1, off sc1" ::"v"(p), "v"(v) : "memory"); }
DI void st2(void* p, unsigned v) { asm volatile("global_store_short %0, %1, off sc1" ::"v"(p), "v"(v) : "memory"); }
DI float smul(float a, float b) { float r; asm("v_mul_f32 %0, %1, %2" : "=v"(r) : "v"(a), "v"(b)); return r; }
DI float sadd(float a, float b) { float r; asm("v_add_f32 %0, %1, %2" : "=v"(r) : "v"(a), "v"(b)); return r; }
struct cplx { float re, im; };
DI cplx cmul(cplx a, cplx b) {
  cplx r;
  asm("v_mul_f32 %0, %2, %4\n\tv_mul_f32 %1, %2, %5\n\tv_fma_f32 %0, -%3, %5, %0\n\tv_fma_f32 %1, %3, %4, %1"
      : "=&v"(r.re), "=&v"(r.im) : "v"(a.re), "v"(a.im), "v"(b.re), "v"(b.im));
  return r;
}

constexpr int K1_LDS = 81920;
constexpr int KG_ROW = 272;
constexpr int KG_W = 32 * KG_ROW;
constexpr int KG_KC = 8 * KG_W;
constexpr int QCH = 24576;

__global__ void __launch_bounds__(512, 4)
k1_prep(const u16* __restrict__ x, const u16* __restrict__ lar, const u16* __restrict__ aim,
        const u16* __restrict__ cre, const u16* __restrict__ cim, const u16* __restrict__ logdt,
        const u16* __restrict__ wq, const u16* __restrict__ wk, const u16* __restrict__ wv,
        const u16* __restrict__ gatew, const u16* __restrict__ wo, char* __restrict__ ws) {
  __shared__ __attribute__((aligned(16))) char smem[K1_LDS];
  const int tid = threadIdx.x, lane = tid & 63, w = __builtin_amdgcn_readfirstlane(tid >> 6);
  const int r32 = lane & 31, h = lane >> 5;
  const int b = blockIdx.x;
  if (b < 288) {
    const int ch = w >> 2, half = w & 1, tp = (w >> 1) & 1, n = 64 * half + lane;
    const int d0 = 2 * b, d = d0 + ch;
    const float ldt = bf2f(logdt[d]), la = bf2f(lar[d * NMODE + n]), aimv = bf2f(aim[d * NMODE + n]);
    const float cr0 = bf2f(cre[d * NMODE + n]), ci0 = bf2f(cim[d * NMODE + n]);
    const float cr1 = bf2f(cre[d * NMODE + (n ^ 64)]), ci1 = bf2f(cim[d * NMODE + (n ^ 64)]);
    char* Vt = smem + w * KG_W;
    char* Wt = Vt + 16 * KG_ROW;
    const float dt = rbf(expf(ldt));
    const float ar = -rbf(expf(la)) * dt, ai = aimv * dt;
    float cm = fmaxf(fmaxf(fabsf(cr0), fabsf(ci0)), fmaxf(fabsf(cr1), fabsf(ci1))) * dt;
#pragma unroll
    for (int o = 32; o >= 1; o >>= 1) cm = fmaxf(cm, __shfl_xor(cm, o));
    int ex = 0;
    if (cm > 0.f && cm < 3.0e38f) ex = (int)((__float_as_uint(cm) >> 23) & 255u) - 127;
    ex = max(-120, min(120, ex));
    const float sc = __uint_as_float((unsigned)(127 - ex) << 23), isc2 = __uint_as_float((unsigned)(127 + ex + 1) << 23);
    const cplx cc = {cr0 * dt * sc, ci0 * dt * sc};
    cplx zp[9];
#pragma unroll
    for (int i = 0; i < 9; i++) {
      const float t = (float)(1 << (2 * tp + i));
      const float mag = __builtin_amdgcn_exp2f((ar * t) * 1.4426950408889634f);
      const float rev = __builtin_amdgcn_fractf((ai * t) * 0.15915494309189535f);
      zp[i] = cplx{mag * __builtin_amdgcn_cosf(rev), mag * __builtin_amdgcn_sinf(rev)};
    }
    float* Kc = (float*)(smem + KG_KC) + (2 * ch + half) * 648;
#pragma unroll
    for (int tl = 0; tl < 2; tl++) {
      const int ti = 2 * tp + tl;
      const int nrows = ti == 0 ? 16 : (ti == 3 ? 9 : 8), toff = ti ? 128 + 128 * ti : 0;
      cplx v[4];
      v[0] = cplx{1.f, 0.f};
      v[1] = zp[tl];
      v[2] = zp[tl + 1];
      v[3] = cmul(v[2], zp[tl]);
#pragma unroll
      for (int g = 0; g < 4; g++) {
#pragma unroll
        for (int i = 0; i < 4; i++) {
          *(unsigned*)(Vt + (4 * g + i) * KG_ROW + lane * 4) = pk_f16(v[i].re, v[i].im);
          if (g < 3) v[i] = (g == 0 && i == 0) ? zp[tl + 2] : cmul(v[i], zp[tl + 2]);
        }
      }
      cplx u[4];
      u[0] = cmul(cc, zp[tl + 7]);
      if (tl == 0 && tp == 0) u[0] = cc;
      u[1] = cmul(u[0], zp[tl + 4]);
      u[2] = cmul(u[0], zp[tl + 5]);
      u[3] = cmul(u[2], zp[tl + 4]);
#pragma unroll
      for (int g = 0; g < 4; g++) {
        if (g < 2 || (g == 2 && nrows > 8) || nrows == 16) {
#pragma unroll
          for (int i = 0; i < 4; i++) {
            *(unsigned*)(Wt + (4 * g + i) * KG_ROW + lane * 4) = pk_f16(u[i].re, -u[i].im);
            if (g < 3) u[i] = cmul(u[i], zp[tl + 6]);
          }
        } else {
#pragma unroll
          for (int i = 0; i < 4; i++) *(unsigned*)(Wt + (4 * g + i) * KG_ROW + lane * 4) = 0u;
        }
      }
      wait_lgkm0();
      const int off = (lane & 15) * KG_ROW + (lane >> 4) * 16;
      f32x4 acc = {0.f, 0.f, 0.f, 0.f};
      f16x8 af[4], bf[4];
#pragma unroll
      for (int st = 0; st < 4; st++) {
        af[st] = *(const f16x8*)(Wt + off + st * 64);
        bf[st] = *(const f16x8*)(Vt + off + st * 64);
      }
      wait_lgkm0();
#pragma unroll
      for (int st = 0; st < 4; st++) acc = __builtin_amdgcn_mfma_f32_16x16x32_f16(af[st], bf[st], acc, 0, 0, 0);
#pragma unroll
      for (int rg = 0; rg < 4; rg++) {
        const int a = 4 * (lane >> 4) + rg, idx = toff + 16 * a + (lane & 15);
        if (a < nrows && idx <= 640) Kc[idx] = acc[rg] * isc2;
      }
    }
    lds_barrier();
    const int tt = tid & 255;
    const float* K0 = (const float*)(smem + KG_KC) + 2 * ch * 648;
    float kv[8];
#pragma unroll
    for (int e = 0; e < 8; e++) {
      const int j = 2047 - (8 * tt + e);
      const unsigned ub = __float_as_uint((float)j);
      const int bits = (int)((ub + 0x7fffu + ((ub >> 16) & 1u)) >> 16);
      const int idx = j <= 256 ? j : 256 + bits - 0x4380;
      kv[e] = sadd(K0[idx], K0[648 + idx]);
    }
    st16((u16*)(ws + WS_KR) + (size_t)d * YLD + 8 * tt,
         u32x4{pk_bf16(kv[0], kv[1]), pk_bf16(kv[2], kv[3]), pk_bf16(kv[4], kv[5]), pk_bf16(kv[6], kv[7])});
  } else if (b < 432) {
    __builtin_amdgcn_s_setprio(2);
    const int bq = b - 288, rb = 2 * (bq & 7) + (bq >> 3) / 9, ct = (bq >> 3) % 9, n0 = ct * 64;
    const u16* A = x + (size_t)rb * 128 * DM;
    const u16* W = wq + n0;
    const int G = lane >> 4, i16 = lane & 15;
    auto src = [&](int p) -> const u16* {
      const int row = 8 * (p & 15) + (lane >> 3);
      if (p < 16) return A + (size_t)row * DM + (((lane & 7) ^ ((row >> 1) & 7)) * 8);
      return W + (size_t)row * DM + (((lane & 7) ^ (((row >> 1) & 1) << 2)) * 8);
    };
    {
      const u16 *s0 = src(w), *s1 = src(w + 8), *s2 = src(w + 16);
#pragma unroll
      for (int kc = 0; kc < 3; kc++) {
        glds16(s0 + kc * 64, smem + kc * QCH + w * 1024);
        glds16(s1 + kc * 64, smem + kc * QCH + (w + 8) * 1024);
        glds16(s2 + (size_t)kc * 64 * DM, smem + kc * QCH + (w + 16) * 1024);
      }
    }
    if (w >= 4) {
      const int L = w - 4;
      const u16* sp[6];
#pragma unroll
      for (int j = 0; j < 6; j++) sp[j] = src(L + 4 * j);
#pragma unroll
      for (int c = 0; c < 9; c++) {
        if (c == 0) wait_vm<6>();
        else if (c == 8) wait_vm<0>();
        else if (c == 1) { if (ct == 0) wait_vm<7>(); else wait_vm<3>(); }
        else if (c == ct + 1 || c == ct + 2) wait_vm<10>();
        else wait_vm<6>();
        lds_barrier();
        if (c >= 1 && c + 2 < 9) {
          char* buf = smem + ((c + 2) % 3) * QCH + L * 1024;
#pragma unroll
          for (int j = 0; j < 6; j++) glds16(sp[j] + (size_t)(c + 2) * (j < 4 ? 64 : 64 * DM), buf + j * 4096);
        }
        if (c == ct) {
          const char* buf = smem + (c % 3) * QCH;
          const int l0 = 32 * L + 8 * G, ra = l0 + (i16 >> 2), rbw = ra + 4;
#pragma unroll
          for (int cj = 0; cj < 4; cj++) {
            const int c16 = 2 * cj + ((i16 >> 1) & 1);
            const s16x4 lo = tr_read(buf + ra * 128 + ((c16 ^ ((ra >> 1) & 7)) * 16) + (i16 & 1) * 8);
            const s16x4 hi = tr_read(buf + rbw * 128 + ((c16 ^ ((rbw >> 1) & 7)) * 16) + (i16 & 1) * 8);
            st16((u16*)(ws + WS_UT) + (size_t)(64 * c + 16 * cj + i16) * YLD + 128 * rb + l0,
                 __builtin_bit_cast(u32x4, __builtin_shufflevector(lo, hi, 0, 1, 2, 3, 4, 5, 6, 7)));
          }
        }
      }
    } else {
      const int arow = 32 * w + r32, aoff = arow * 128, asw = (arow >> 1) & 7;
      const int q4 = i16 >> 2, p4 = i16 & 3;
      const int bo0 = 16384 + (8 * (G >> 1) + q4) * 128 + (((2 * (G & 1) + (p4 >> 1)) ^ (((q4 >> 1) & 1) << 2)) * 16) + (p4 & 1) * 8;
      const int bo1 = bo0 ^ 64;
      f32x16 acc0 = zero16(), acc1 = zero16();
#pragma unroll
      for (int kc = 0; kc < 9; kc++) {
        if (kc == 0) wait_vm<6>();
        else if (kc == 1) wait_vm<3>();
        else if (kc == 2) wait_vm<0>();
        lds_barrier();
        const char* buf = smem + (kc % 3) * QCH;
        bf16x8 fa[4], fb0[4], fb1[4];
#pragma unroll
        for (int s = 0; s < 4; s++) {
          fa[s] = *(const bf16x8*)(buf + aoff + (((2 * s + h) ^ asw) * 16));
          s16x4 lo = tr_read(buf + bo0 + 2048 * s), hi = tr_read(buf + bo0 + 2048 * s + 512);
          fb0[s] = __builtin_shufflevector(lo, hi, 0, 1, 2, 3, 4, 5, 6, 7);
          lo = tr_read(buf + bo1 + 2048 * s);
          hi = tr_read(buf + bo1 + 2048 * s + 512);
          fb1[s] = __builtin_shufflevector(lo, hi, 0, 1, 2, 3, 4, 5, 6, 7);
        }
#pragma unroll
        for (int s = 0; s < 4; s++) {
          acc0 = MFMA32(fa[s], fb0[s], acc0);
          acc1 = MFMA32(fa[s], fb1[s], acc1);
        }
      }
      const float c = 0.23570226039551584f * 1.4426950408889634f;
      u16* Qs = (u16*)(ws + WS_Q) + (size_t)(128 * rb + 32 * w + 4 * h) * DM + n0 + r32;
#pragma unroll
      for (int i = 0; i < 16; i++) {
        st2(Qs + ((i & 3) + 8 * (i >> 2)) * DM, pk_bf16(acc0[i] * c, 0.f));
        st2(Qs + ((i & 3) + 8 * (i >> 2)) * DM + 32, pk_bf16(acc1[i] * c, 0.f));
      }
    }
  } else if (b < 496) {
    const int T = b - 432;
    const u16* xa = x + (size_t)T * 32 * DM;
    for (int p = w; p < 32; p += 8) {
      const int row = 8 * (p & 3) + (lane >> 3);
      glds16(xa + (size_t)row * DM + (p >> 2) * 64 + (((lane & 7) ^ ((row >> 1) & 7)) * 8), smem + p * 1024);
    }
    char* bres = smem + 32768;
    for (int k = tid; k < DM; k += 512) {
      const u32x4* sk = (const u32x4*)(wk + k * DHEAD);
      const u32x4* sv = (const u32x4*)(wv + k * DHEAD);
      const u32x4 k0 = sk[0], k1 = sk[1], v0 = sv[0], v1 = sv[1];
      const unsigned k2 = ((const unsigned*)sk)[8], v2 = ((const unsigned*)sv)[8], gw = gatew[k];
      u32x4* dst = (u32x4*)(bres + k * 80);
      dst[0] = k0;
      dst[1] = k1;
      dst[2] = u32x4{k2, gw, v0[0], v0[1]};
      dst[3] = u32x4{v0[2], v0[3], v1[0], v1[1]};
      dst[4] = u32x4{v1[2], v1[3], v2, 0u};
    }
    if (tid < 2) *(u32x4*)(bres + DM * 80 + tid * 16) = u32x4{0u, 0u, 0u, 0u};
    const int isv = w & 1, kq = w >> 1;
    bf16x8 atail[4];
    if (kq == 3) {
#pragma unroll
      for (int s = 0; s < 4; s++) atail[s] = *(const bf16x8*)(xa + (size_t)r32 * DM + 512 + 16 * s + 8 * h);
    }
    wait_vm<0>();
    __syncthreads();
    f32x16 acc = zero16();
    {
      const int G = lane >> 4, i16 = lane & 15;
      const char* bb = bres + (144 * kq + 8 * (G >> 1) + (i16 >> 2)) * 80 + (20 * isv + 16 * (G & 1) + 4 * (i16 & 3)) * 2;
      const char* ab = smem + r32 * 128;
      const int asw = (r32 >> 1) & 7;
#pragma unroll
      for (int i = 0; i < 9; i++) {
        const int st = 9 * kq + i;
        bf16x8 av = *(const bf16x8*)(ab + ((st >> 2) & 7) * 4096 + (((2 * (st & 3) + h) ^ asw) * 16));
        if (i >= 5 && kq == 3) av = atail[i - 5];
        s16x4 lo = tr_read(bb + 16 * i * 80), hi = tr_read(bb + (16 * i + 4) * 80);
        const bf16x8 wv_ = __builtin_shufflevector(lo, hi, 0, 1, 2, 3, 4, 5, 6, 7);
        acc = isv ? MFMA32(av, wv_, acc) : MFMA32(wv_, av, acc);
      }
    }
    lds_barrier();
    float* red = (float*)smem;
    if (w >= 2) {
#pragma unroll
      for (int g = 0; g < 4; g++)
        *(f32x4*)(red + (((w - 2) * 4 + g) * 64 + lane) * 4) = f32x4{acc[4 * g], acc[4 * g + 1], acc[4 * g + 2], acc[4 * g + 3]};
    }
    lds_barrier();
    if (w < 2) {
#pragma unroll
      for (int pq = 0; pq < 3; pq++) {
#pragma unroll
        for (int g = 0; g < 4; g++) {
          const f32x4 pv = *(const f32x4*)(red + (((2 * pq + w) * 4 + g) * 64 + lane) * 4);
#pragma unroll
          for (int e = 0; e < 4; e++) acc[4 * g + e] = sadd(acc[4 * g + e], pv[e]);
        }
      }
      char* blk = ws + WS_KV + (size_t)(T >> 2) * 9216;
      const int wr = T & 3;
      if (w == 0) {
        char* t0 = blk + wr * 1024;
        st8(t0 + r32 * 16 + 8 * h, u32x2{pk_bf16(acc[0], acc[1]), pk_bf16(acc[2], acc[3])});
        st8(t0 + (32 + r32) * 16 + 8 * h, u32x2{pk_bf16(acc[4], acc[5]), pk_bf16(acc[6], acc[7])});
        if (!h) {
          st4(blk + 4096 + wr * 128 + r32 * 4, pk_bf16(acc[8], acc[9]));
          const float z = rbf(acc[10]);
          st4((float*)(ws + WS_G) + 32 * T + r32, __float_as_uint(rbf(1.0f / (1.0f + expf(-z)))));
        }
      } else if (r32 < DHEAD) {
        char* t0 = blk + 4608 + wr * 1152 + h * 288 + r32 * 16;
        st16(t0, u32x4{pk_bf16(acc[0], acc[1]), pk_bf16(acc[2], acc[3]), pk_bf16(acc[4], acc[5]), pk_bf16(acc[6], acc[7])});
        st16(t0 + 576, u32x4{pk_bf16(acc[8], acc[9]), pk_bf16(acc[10], acc[11]), pk_bf16(acc[12], acc[13]), pk_bf16(acc[14], acc[15])});
      }
    }
  } else {
    const int t2 = tid & 127;
    u16* TW = (u16*)(smem + (w >> 1) * 9472);
#pragma unroll
    for (int rnd = 0; rnd < 2; rnd++) {
      const int ul = 4 * rnd + (w >> 1), unit = 6 * (b - 496) + ul, ks = unit >> 1, c0 = (unit & 1) * 288;
      if (ul < 6) {
#pragma unroll
        for (int j = 0; j < 5; j++) {
          const int e = t2 + 128 * j;
          if (e < 576)
            *(bf16x8*)(TW + (e / 36) * 296 + (e % 36) * 8) = *(const bf16x8*)(wo + (size_t)(16 * ks + e / 36) * DM + c0 + (e % 36) * 8);
        }
      }
      lds_barrier();
      if (ul < 6) {
#pragma unroll
        for (int j = 0; j < 5; j++) {
          const int e = t2 + 128 * j;
          if (e < 576) {
            const int ctl = e / 144, q = e % 144;
            const int hh = q < 128 ? (q >> 5) & 1 : (q >> 3) & 1;
            const int col = 72 * ctl + (q < 128 ? 32 * (q >> 6) + (q & 31) : 64 + (q & 7));
            unsigned o4[4];
#pragma unroll
            for (int i = 0; i < 4; i++)
              o4[i] = (unsigned)TW[(8 * hh + 2 * i) * 296 + col] | ((unsigned)TW[(8 * hh + 2 * i + 1) * 296 + col] << 16);
            st16(ws + WS_WP + ((size_t)((unit & 1) * 4 + ctl) * 36 + ks) * 2304 + q * 16, u32x4{o4[0], o4[1], o4[2], o4[3]});
          }
        }
      }
      lds_barrier();
    }
  }
}

constexpr int AT_C = 73728, K2_LDS = 81920;
constexpr int CV_COPY = 4 * 4160;
constexpr int CV2_CH = 40960;

DI u32x4 exp_pack(const f32x16& s, int o) {
  u32x4 p;
#pragma unroll
  for (int i = 0; i < 4; i++)
    p[i] = pk_bf16(__builtin_amdgcn_exp2f(s[o + 2 * i]), __builtin_amdgcn_exp2f(s[o + 2 * i + 1]));
  return p;
}
DI float tile_max(const f32x16& s) {
  float m = s[0];
#pragma unroll
  for (int i = 1; i < 16; i++) m = fmaxf(m, s[i]);
  return m;
}
DI void set_shift(u32x4& q1, float m, int h) {
  const float nm = -m, hi = rbf(nm), r1 = nm - hi, mid = rbf(r1), lo = rbf(r1 - mid);
  if (!h) {
    q1[1] = pk_bf16(hi, mid);
    q1[2] = pk_bf16(lo, 0.f);
  }
}

template <int NPER>
DI void conv_pass(char* __restrict__ ws, char* smem, int dbase, int w, int tid, int lane) {
  constexpr int TPC = NPER * 64;
  const int r32 = lane & 31, h = lane >> 5;
  const int ch = w / NPER, wq = w % NPER, tt = tid % TPC;
  const int d = dbase + ch;
  char* base = smem + ch * CV2_CH;
  __builtin_amdgcn_s_setprio(3);
  if (tt < 256) {
    const u32x4* kr = (const u32x4*)((const u16*)(ws + WS_KR) + (size_t)d * YLD);
    const u32x4 ka = kr[tt];
    u32x2 kb = *(const u32x2*)(kr + tt + 1);
    if (tt == 255) kb = u32x2{0u, 0u};
    const u32x4 uv = ((const u32x4*)((const u16*)(ws + WS_UT) + (size_t)d * YLD))[tt];
    const unsigned dw[6] = {ka[0], ka[1], ka[2], ka[3], kb[0], kb[1]};
#pragma unroll
    for (int sft = 0; sft < 4; sft++) {
      u32x4 v;
#pragma unroll
      for (int e = 0; e < 4; e++)
        v[e] = (sft & 1) ? __builtin_amdgcn_alignbit(dw[(sft >> 1) + e + 1], dw[(sft >> 1) + e], 16) : dw[(sft >> 1) + e];
      char* cpy = base + (3 - sft) * 4160;
      *(u32x4*)(cpy + tt * 16) = v;
      if (tt < 4) *(u32x4*)(cpy + 4096 + tt * 16) = u32x4{0u, 0u, 0u, 0u};
    }
    if (tt < 155) *(u32x4*)(base + CV_COPY + tt * 16) = u32x4{0u, 0u, 0u, 0u};
    *(u32x4*)(base + CV_COPY + (31 + (tt >> 2)) * 80 + (tt & 3) * 16) = uv;
  }
  __syncthreads();
  if (NPER == 4) __builtin_amdgcn_s_setprio(0);
  int k0, k1;
  if (NPER == 4) {
    k0 = wq == 0 ? 0 : (wq == 1 ? 12 : (wq == 2 ? 24 : 40));
    k1 = wq == 0 ? 12 : (wq == 1 ? 24 : (wq == 2 ? 40 : 64));
  } else {
    k0 = wq < 6 ? 6 * wq : (wq == 6 ? 40 : 52);
    k1 = wq < 5 ? 6 * wq + 6 : (wq == 5 ? 40 : (wq == 6 ? 52 : 64));
  }
  const char* bp = base + (r32 & 3) * 4160 + (511 + 2 * h - (r32 >> 2)) * 8;
  const char* ap = base + CV_COPY + (r32 + 31) * 80 + h * 16;
  f32x16 acc0 = zero16(), acc1 = zero16();
  for (int kb = k0; kb < k1; kb += 2) {
    bf16x8 t0[2], t1[2], a1a[2], a1b[2];
#pragma unroll
    for (int i = 0; i < 2; i++) {
      const char* tp = bp - (kb + i) * 64;
      const u32x2 ta = *(const u32x2*)(tp), tb = *(const u32x2*)(tp + 8);
      const u32x2 tc = *(const u32x2*)(tp + 32), td = *(const u32x2*)(tp + 40);
      t0[i] = asbf(u32x4{ta[0], ta[1], tb[0], tb[1]});
      t1[i] = asbf(u32x4{tc[0], tc[1], td[0], td[1]});
      const char* a1p = ap + (32 - kb - i) * 80;
      a1a[i] = *(const bf16x8*)(a1p);
      a1b[i] = *(const bf16x8*)(a1p + 32);
    }
    if (kb < 32) {
      bf16x8 a0a[2], a0b[2];
#pragma unroll
      for (int i = 0; i < 2; i++) {
        const char* a0p = ap - (kb + i) * 80;
        a0a[i] = *(const bf16x8*)(a0p);
        a0b[i] = *(const bf16x8*)(a0p + 32);
      }
#pragma unroll
      for (int i = 0; i < 2; i++) {
        acc1 = MFMA32(a1a[i], t0[i], acc1);
        acc0 = MFMA32(a0a[i], t0[i], acc0);
        acc1 = MFMA32(a1b[i], t1[i], acc1);
        acc0 = MFMA32(a0b[i], t1[i], acc0);
      }
    } else {
#pragma unroll
      for (int i = 0; i < 2; i++) {
        acc1 = MFMA32(a1a[i], t0[i], acc1);
        acc1 = MFMA32(a1b[i], t1[i], acc1);
      }
    }
  }
  __builtin_amdgcn_s_setprio(3);
  __syncthreads();
  float* red = (float*)base;
#pragma unroll
  for (int g = 0; g < 4; g++) {
    *(f32x4*)(red + ((wq * 8 + g) * 64 + lane) * 4) = f32x4{acc0[4 * g], acc0[4 * g + 1], acc0[4 * g + 2], acc0[4 * g + 3]};
    *(f32x4*)(red + ((wq * 8 + 4 + g) * 64 + lane) * 4) = f32x4{acc1[4 * g], acc1[4 * g + 1], acc1[4 * g + 2], acc1[4 * g + 3]};
  }
  __syncthreads();
  u16* yT = (u16*)(ws + WS_YT) + (size_t)d * 64;
#pragma unroll
  for (int it = 0; it < 8 / NPER; it++) {
    const int tl = NPER == 4 ? it : wq >> 2, g = wq & 3;
    f32x4 sum = *(const f32x4*)(red + ((tl * 4 + g) * 64 + lane) * 4);
#pragma unroll
    for (int ww = 1; ww < NPER; ww++) sum += *(const f32x4*)(red + ((ww * 8 + tl * 4 + g) * 64 + lane) * 4);
#pragma unroll
    for (int e = 0; e < 4; e++) {
      const int I = 32 * tl + e + 8 * g + 4 * h;
      st2(yT + (size_t)(I >> 1) * (DM * 64) + (I & 1) * 32 + r32, pk_bf16(sum[e], 0.f));
    }
  }
}

__global__ void __launch_bounds__(512, 4)
k2_attn_conv(char* __restrict__ ws) {
  __shared__ __attribute__((aligned(16))) char smem[K2_LDS];
  const int tid = threadIdx.x, lane = tid & 63, w = __builtin_amdgcn_readfirstlane(tid >> 6);
  const int r32 = lane & 31, h = lane >> 5;
  if (blockIdx.x < 256) {
    const int q = blockIdx.x * 8 + w;
    const u16* qp = (const u16*)(ws + WS_Q) + (size_t)q * DM + r32 * DHEAD;
    u32x4 q0;
    unsigned q16;
    asm volatile("global_load_dwordx4 %0, %2, off\n\tglobal_load_dword %1, %3, off"
                 : "=&v"(q0), "=&v"(q16) : "v"(qp + 8 * h), "v"(qp + 16) : "memory");
    const char* src = ws + WS_KV + w * 1024 + lane * 16;
    auto issue = [&](int j) { glds16(src + j * 8192, smem + w * 1024 + (j >= 9 ? j - 9 : j) * 8192); };
    issue(0);
    issue(1);
    issue(2);
    if (tid < 8) {
      *(u32x4*)(smem + AT_C + tid * 576) = u32x4{0x3f803f80u, 0x3f803f80u, 0x3f803f80u, 0x3f803f80u};
      *(u32x4*)(smem + AT_C + tid * 576 + 16) = u32x4{0u, 0u, 0u, 0u};
      if (tid < 4) *(unsigned*)(smem + AT_C + 32 + tid * 128) = 0u;
    }
    asm volatile("s_waitcnt vmcnt(0) lgkmcnt(0)" : "+v"(q0), "+v"(q16)::"memory");
    barrier_raw();
    u32x4 q1 = {h ? 0u : q16, 0u, 0u, 0u};
    const unsigned kc1 = h ? 0u : 0x3f803f80u, kc2 = h ? 0u : 0x00003f80u;
    const int k1step = h ? 0 : 9216, vstep = r32 < DHEAD ? 9216 : 0;
    const int k0base = lane * 16;
    const int k1base = h ? AT_C + 32 : 4096 + r32 * 4;
    const int vbase = r32 < DHEAD ? 4608 + h * 288 + r32 * 16 : (r32 == DHEAD ? AT_C : AT_C + 16);
    auto ldk0 = [&](int a, int t) { return *(const bf16x8*)(smem + a + t * 1024); };
    auto ldk1 = [&](int a, int t) { return asbf(u32x4{*(const unsigned*)(smem + a + t * 128), kc1, kc2, 0u}); };
    auto ldv = [&](int a, int t, int s) { return *(const bf16x8*)(smem + a + t * 1152 + s * 576); };

    bf16x8 k0 = ldk0(k0base, 0), k1 = ldk1(k1base, 0);
    {
      f32x16 s = MFMA32(k0, asbf(q0), zero16());
      s = MFMA32(k1, asbf(q1), s);
      float m = tile_max(s);
      m = fmaxf(m, __shfl_xor(m, 32));
      set_shift(q1, m, h);
    }
    f32x16 o = zero16();
    f32x16 s = MFMA32(k0, asbf(q0), zero16());
    s = MFMA32(k1, asbf(q1), s);
    k0 = ldk0(k0base, 1);
    k1 = ldk1(k1base, 1);
    bf16x8 v0 = ldv(vbase, 0, 0), v1 = ldv(vbase, 0, 1);
    int ak0 = k0base, ak1 = k1base, av = vbase;
#pragma unroll
    for (int c = 0; c < 16; c++) {
      const int ns = c == 15 ? 7 : ((c + 1) & 7);
      const int nk0 = k0base + ns * 9216, nk1 = k1base + ns * k1step, nv = vbase + ns * vstep;
#pragma unroll
      for (int tt = 0; tt < 4; tt++) {
        const int T = 4 * c + tt;
        u32x4 p0, p1;
        p0[0] = pk_bf16(__builtin_amdgcn_exp2f(s[0]), __builtin_amdgcn_exp2f(s[1]));
        p0[1] = pk_bf16(__builtin_amdgcn_exp2f(s[2]), __builtin_amdgcn_exp2f(s[3]));
        SCHED_FENCE();
        f32x16 sn = MFMA32(k0, asbf(q0), zero16());
        SCHED_FENCE();
        p0[2] = pk_bf16(__builtin_amdgcn_exp2f(s[4]), __builtin_amdgcn_exp2f(s[5]));
        p0[3] = pk_bf16(__builtin_amdgcn_exp2f(s[6]), __builtin_amdgcn_exp2f(s[7]));
        SCHED_FENCE();
        o = MFMA32(v0, asbf(p0), o);
        SCHED_FENCE();
        p1[0] = pk_bf16(__builtin_amdgcn_exp2f(s[8]), __builtin_amdgcn_exp2f(s[9]));
        p1[1] = pk_bf16(__builtin_amdgcn_exp2f(s[10]), __builtin_amdgcn_exp2f(s[11]));
        SCHED_FENCE();
        sn = MFMA32(k1, asbf(q1), sn);
        if (T == 6) {
          wait_vm<2>();
          barrier_raw();
        } else if (T == 22 || T == 30 || T == 38 || T == 54) {
          wait_vm<0>();
          barrier_raw();
        }
        if (T < 6) issue(3 + T);
        if (T >= 22 && T < 27) issue(T - 13);
        if (T == 30 || T == 31) issue(T - 16);
        if (T == 38 || T == 39) issue(T - 22);
        k0 = tt < 2 ? ldk0(ak0, tt + 2) : ldk0(nk0, tt - 2);
        k1 = tt < 2 ? ldk1(ak1, tt + 2) : ldk1(nk1, tt - 2);
        v0 = tt < 3 ? ldv(av, tt + 1, 0) : ldv(nv, 0, 0);
        const bf16x8 vn1 = tt < 3 ? ldv(av, tt + 1, 1) : ldv(nv, 0, 1);
        SCHED_FENCE();
        p1[2] = pk_bf16(__builtin_amdgcn_exp2f(s[12]), __builtin_amdgcn_exp2f(s[13]));
        p1[3] = pk_bf16(__builtin_amdgcn_exp2f(s[14]), __builtin_amdgcn_exp2f(s[15]));
        SCHED_FENCE();
        o = MFMA32(v1, asbf(p1), o);
        SCHED_FENCE();
        v1 = vn1;
        s = sn;
      }
      ak0 = nk0;
      ak1 = nk1;
      av = nv;
    }
    bool bad = false;
#pragma unroll
    for (int i = 0; i < 12; i++) bad |= !(fabsf(o[i]) < 1.0e30f);
    if (__any(bad)) {
      const char* g = ws + WS_KV;
      auto gk0 = [&](int t) { return *(const bf16x8*)(g + (t >> 2) * 9216 + (t & 3) * 1024 + lane * 16); };
      auto gk1 = [&](int t) {
        const unsigned v = *(const unsigned*)(g + (t >> 2) * 9216 + 4096 + (t & 3) * 128 + r32 * 4);
        return asbf(u32x4{h ? 0u : v, kc1, kc2, 0u});
      };
      auto gv = [&](int t, int st) {
        const int dd = r32 < DHEAD ? r32 : 0;
        u32x4 v = *(const u32x4*)(g + (t >> 2) * 9216 + 4608 + (t & 3) * 1152 + st * 576 + h * 288 + dd * 16);
        const unsigned cv = r32 == DHEAD ? 0x3f803f80u : 0u;
        if (r32 >= DHEAD) v = u32x4{cv, cv, cv, cv};
        return asbf(v);
      };
      q1[1] = 0u;
      q1[2] = 0u;
      float m = -__builtin_inff();
      for (int t = 0; t < 64; t++) {
        f32x16 st = MFMA32(gk0(t), asbf(q0), zero16());
        st = MFMA32(gk1(t), asbf(q1), st);
        m = fmaxf(m, tile_max(st));
      }
      m = fmaxf(m, __shfl_xor(m, 32));
      set_shift(q1, m, h);
      o = zero16();
      for (int t = 0; t < 64; t++) {
        f32x16 st = MFMA32(gk0(t), asbf(q0), zero16());
        st = MFMA32(gk1(t), asbf(q1), st);
        o = MFMA32(gv(t, 0), asbf(exp_pack(st, 0)), o);
        o = MFMA32(gv(t, 1), asbf(exp_pack(st, 8)), o);
      }
    }
    const float l = __shfl(o[10], r32);
    const float il = 1.0f / l;
    unsigned* op = (unsigned*)((u16*)(ws + WS_O) + (size_t)q * DM + r32 * DHEAD);
    st4(op + 2 * h, pk_bf16(o[0] * il, o[1] * il));
    st4(op + 2 * h + 1, pk_bf16(o[2] * il, o[3] * il));
    st4(op + 4 + 2 * h, pk_bf16(o[4] * il, o[5] * il));
    st4(op + 4 + 2 * h + 1, pk_bf16(o[6] * il, o[7] * il));
    if (!h) st4(op + 8, pk_bf16(o[8] * il, o[9] * il));
  } else {
    const int cb = blockIdx.x - 256;
    conv_pass<4>(ws, smem, 2 * cb, w, tid, lane);
    if (cb < 64) {
      wait_lgkm0();
      barrier_raw();
      conv_pass<8>(ws, smem, 512 + cb, w, tid, lane);
    }
  }
}

constexpr int GCH = 17408, K3_G = 9 * GCH, K3_LDS = K3_G + 512;

__global__ void __launch_bounds__(768, 3)
k3_out(const u16* __restrict__ x, const u16* __restrict__ dskip, const char* __restrict__ ws, u16* __restrict__ out) {
  __shared__ __attribute__((aligned(16))) char smem[K3_LDS];
  const int tid = threadIdx.x, lane = tid & 63, w = __builtin_amdgcn_readfirstlane(tid >> 6);
  const int r32 = lane & 31, h = lane >> 5;
  const int rb = 4 * (blockIdx.x & 7) + ((blockIdx.x >> 3) & 3), ct = blockIdx.x >> 5, n0 = ct * 72;
  if (w >= 6) {
    const int L = w - 6;
    const u16* A = (const u16*)(ws + WS_O) + (size_t)rb * 64 * DM;
    const char* W = ws + WS_WP + (size_t)ct * 82944;
    if (L == 5 && lane < 16) glds16(ws + WS_G + (size_t)rb * 256 + lane * 16, smem + K3_G);
    if (L == 5 && lane < 9) glds16(dskip + n0 + lane * 8, smem + K3_G + 256);
    const char* src[3];
    int step[3];
#pragma unroll
    for (int j = 0; j < 3; j++) {
      const int pp = L + 6 * j > 16 ? L : L + 6 * j;
      if (pp < 8) {
        const int row = 8 * pp + (lane >> 3);
        src[j] = (const char*)(A + (size_t)row * DM + (((lane & 7) ^ ((row >> 1) & 7)) * 8));
        step[j] = 128;
      } else {
        src[j] = W + (pp - 8) * 1024 + lane * 16;
        step[j] = 9216;
      }
    }
    auto issue = [&](int kc) {
#pragma unroll
      for (int j = 0; j < 3; j++)
        glds16(src[j] + kc * step[j], smem + kc * GCH + (L + 6 * j > 16 ? L : L + 6 * j) * 1024);
    };
#pragma unroll
    for (int kc = 0; kc < 4; kc++) issue(kc);
    wait_vm<6>();
    barrier_raw();
    issue(4);
    issue(5);
    wait_vm<6>();
    barrier_raw();
    issue(6);
    issue(7);
    issue(8);
    wait_vm<9>();
    barrier_raw();
#pragma unroll
    for (int j = 0; j < 3; j++) {
      const int p = L + 6 * j;
      if (p < 9) {
        const int dd = 8 * p + (lane >> 3);
        glds16((const u16*)(ws + WS_YT) + ((size_t)rb * DM + n0 + dd) * 64 + (lane & 7) * 8, smem + p * 1024);
      } else {
        const int P = 64 * (p - 9) + lane, l = P / 9;
        glds16(x + (size_t)(64 * rb + l) * DM + n0 + (P - 9 * l) * 8, smem + GCH + (p - 9) * 1024);
      }
    }
    wait_vm<3>();
    barrier_raw();
    wait_vm<0>();
  } else {
    const int wr = w / 3, wc = w % 3;
    const int arow = 32 * wr + r32, aoff = arow * 128, asw = (arow >> 1) & 7;
    const int boff = 8192 + wc * 1024 + (wc == 2 ? ((lane & 7) + 8 * h) * 16 : lane * 16);
    bf16x8 fa[2][4], fb[2][4];
    auto ld = [&](int kc) {
      const char* buf = smem + kc * GCH;
#pragma unroll
      for (int s = 0; s < 4; s++) {
        fa[kc & 1][s] = *(const bf16x8*)(buf + aoff + (((2 * s + h) ^ asw) * 16));
        fb[kc & 1][s] = *(const bf16x8*)(buf + boff + 2304 * s);
      }
    };
    f32x16 acc = zero16();
#pragma unroll
    for (int kc = 0; kc < 9; kc++) {
      if (kc == 0 || kc == 2 || kc == 4 || kc == 6) {
        lds_barrier();
        ld(kc);
      }
      if (kc == 0 || kc == 2 || kc == 4 || kc == 6 || kc == 7) ld(kc + 1);
#pragma unroll
      for (int s = 0; s < 4; s++) acc = MFMA32(fa[kc & 1][s], fb[kc & 1][s], acc);
    }
    const int dl = 32 * wc + r32;
    if (dl < 72) {
      u16* bt = (u16*)(smem + 2 * GCH) + (32 * wr + 4 * h) * 72 + dl;
#pragma unroll
      for (int i = 0; i < 16; i++) bt[((i & 3) + 8 * (i >> 2)) * 72] = (u16)pk_bf16(acc[i], 0.f);
    }
    wait_lgkm0();
  }
  barrier_raw();
  if (tid < 576) {
    const int l = tid / 9, c8 = (tid - 9 * l) * 8;
    const u32x4 bv = *(const u32x4*)(smem + 2 * GCH + l * 144 + c8 * 2);
    const u32x4 uv = *(const u32x4*)(smem + GCH + l * 144 + c8 * 2);
    const u32x4 dv = *(const u32x4*)(smem + K3_G + 256 + c8 * 2);
    const float gg = *(const float*)(smem + K3_G + l * 4), omg = rbf(1.0f - gg);
    float r[8];
#pragma unroll
    for (int e = 0; e < 8; e++) {
      const float y = bf2f(*(const u16*)(smem + (c8 + e) * 128 + l * 2));
      const float u = (e & 1) ? bfhi(uv[e >> 1]) : bflo(uv[e >> 1]);
      const float ds = (e & 1) ? bfhi(dv[e >> 1]) : bflo(dv[e >> 1]);
      const float bb = (e & 1) ? bfhi(bv[e >> 1]) : bflo(bv[e >> 1]);
      const float a = rbf(sadd(y, rbf(smul(u, ds))));
      r[e] = sadd(rbf(smul(gg, a)), rbf(smul(omg, bb)));
    }
    st16(out + (size_t)(64 * rb + l) * DM + n0 + c8,
         u32x4{pk_bf16(r[0], r[1]), pk_bf16(r[2], r[3]), pk_bf16(r[4], r[5]), pk_bf16(r[6], r[7])});
  }
}

extern "C" void kernel_launch(void* const* d_in, const int* in_sizes, int n_in, void* d_out, int out_size,
                              void* d_ws, size_t ws_size, hipStream_t stream) {
  const u16* x = (const u16*)d_in[0];
  const u16* lar = (const u16*)d_in[1];
  const u16* aim = (const u16*)d_in[2];
  const u16* cre = (const u16*)d_in[3];
  const u16* cim = (const u16*)d_in[4];
  const u16* logdt = (const u16*)d_in[5];
  const u16* dskip = (const u16*)d_in[6];
  const u16* wq = (const u16*)d_in[8];
  const u16* wk = (const u16*)d_in[9];
  const u16* wv = (const u16*)d_in[10];
  const u16* wo = (const u16*)d_in[11];
  const u16* gatew = (const u16*)d_in[12];
  char* ws = (char*)d_ws;
  k1_prep<<<508, 512, 0, stream>>>(x, lar, aim, cre, cim, logdt, wq, wk, wv, gatew, wo, ws);
  k2_attn_conv<<<512, 512, 0, stream>>>(ws);
  k3_out<<<256, 768, 0, stream>>>(x, dskip, ws, (u16*)d_out);
}
```
